```python
import torch

def _softmax(x, axis=-1):
    return torch.softmax(x, dim=axis)
def _gelu(x):
    return torch.nn.functional.gelu(x, approximate="tanh")
def _silu(x):
    return torch.nn.functional.silu(x)
def _sigmoid(x):
    return torch.sigmoid(x)
def _layernorm(x, axis=-1, eps=1e-5):
    m = x.mean(dim=axis, keepdim=True)
    v = x.var(dim=axis, keepdim=True, unbiased=False)
    return (x - m) / torch.sqrt(v + eps)
def _scatter_add(tgt, idx, src, axis=0):
    return tgt.clone().index_add_(axis, idx, src)
def _segment_sum(src, seg, n):
    out = torch.zeros((n,) + src.shape[1:], dtype=src.dtype)
    return out.index_add_(0, seg, src)
def _pad(x, pw):
    flat = tuple(v for pair in reversed(pw) for v in pair)
    return torch.nn.functional.pad(x, flat)

kernel_name = 'standalone__gqa_softmax__near_ridge'

def setup_inputs():

    _w = (lambda a: (lambda t: t.to(torch.bfloat16) if t.is_floating_point() else t)(torch.as_tensor(a)))
    B, Hq, Hkv, Sq, Sk, D = 8, 32, 4, 32, 2048, 128
    return {
        "q": _w((lambda *s: torch.randn(*s, dtype=torch.bfloat16) * 0.8)(B, Hq, Sq, D)),
        "k": _w((lambda *s: torch.randn(*s, dtype=torch.bfloat16) * 0.8)(B, Hkv, Sk, D)),
        "v": _w((lambda *s: torch.randn(*s, dtype=torch.bfloat16) * 0.05)(B, Hkv, Sk, D)),
    }

def reference(q, k, v):
    B, Hq, Sq, D = q.shape
    Hkv = k.shape[1]
    rep = Hq // Hkv
    k = (lambda x, n, axis: torch.repeat_interleave(x, n, dim=axis))(k, rep, axis=1)
    v = (lambda x, n, axis: torch.repeat_interleave(x, n, dim=axis))(v, rep, axis=1)
    scale = D ** -0.5
    scores = torch.einsum("bhqd,bhkd->bhqk", q, k) * scale
    p = _softmax(scores, axis=-1)
    return torch.einsum("bhqk,bhkd->bhqd", p, v)
```

```cpp
#include <hip/hip_runtime.h>
#include <hip/hip_bf16.h>

typedef float f32x16 __attribute__((ext_vector_type(16)));
typedef float f32x4 __attribute__((ext_vector_type(4)));
typedef float f32x2 __attribute__((ext_vector_type(2)));
typedef __bf16 bf16x8 __attribute__((ext_vector_type(8)));
typedef __bf16 bf16x4 __attribute__((ext_vector_type(4)));
typedef __bf16 bf16x2 __attribute__((ext_vector_type(2)));
typedef short s16x4 __attribute__((ext_vector_type(4)));
typedef unsigned u32x4 __attribute__((ext_vector_type(4)));
typedef unsigned u32x2 __attribute__((ext_vector_type(2)));
#define LAS __attribute__((address_space(3)))
#define DI __device__ __forceinline__

constexpr int B_ = 8, HQ = 32, HKV = 4, SQ = 32, SK = 2048, D = 128;
constexpr int NW = 4;
constexpr int NSLOT = 4;
constexpr int SLOT = 8192;
constexpr int TPW = SK / 32 / NW;
constexpr float CSC = 0.088388347648318447f * 1.4426950408889634f;

DI unsigned cvtpk(float lo, float hi) {
  f32x2 v = {lo, hi}; bf16x2 b = __builtin_convertvector(v, bf16x2); return __builtin_bit_cast(unsigned, b);
}

DI void dma_k(unsigned ldsa, const char* gbase, unsigned v0, unsigned v1, unsigned v2, unsigned v3) {
  asm volatile(
      "s_waitcnt lgkmcnt(0)\n\t"
      "s_mov_b32 m0, %0\n\ts_nop 0\n\t"
      "global_load_lds_dwordx4 %2, %1 offset:-4096\n\t"
      "global_load_lds_dwordx4 %2, %1 offset:-3072\n\t"
      "global_load_lds_dwordx4 %3, %1 offset:-2048\n\t"
      "global_load_lds_dwordx4 %3, %1 offset:-1024\n\t"
      "global_load_lds_dwordx4 %4, %1\n\t"
      "global_load_lds_dwordx4 %4, %1 offset:1024\n\t"
      "global_load_lds_dwordx4 %5, %1 offset:2048\n\t"
      "global_load_lds_dwordx4 %5, %1 offset:3072\n\t"
      :: "s"(ldsa), "s"(gbase), "v"(v0), "v"(v1), "v"(v2), "v"(v3) : "memory");
}
DI void dma_v(unsigned ldsa, const char* gbase, unsigned v0) {
  asm volatile(
      "s_waitcnt lgkmcnt(0)\n\t"
      "s_mov_b32 m0, %0\n\ts_nop 0\n\t"
      "global_load_lds_dwordx4 %2, %1 offset:-4096\n\t"
      "global_load_lds_dwordx4 %2, %1 offset:-3072\n\t"
      "global_load_lds_dwordx4 %2, %1 offset:-2048\n\t"
      "global_load_lds_dwordx4 %2, %1 offset:-1024\n\t"
      "global_load_lds_dwordx4 %2, %1\n\t"
      "global_load_lds_dwordx4 %2, %1 offset:1024\n\t"
      "global_load_lds_dwordx4 %2, %1 offset:2048\n\t"
      "global_load_lds_dwordx4 %2, %1 offset:3072\n\t"
      :: "s"(ldsa), "s"(gbase), "v"(v0) : "memory");
}
#define WAITVM(n) asm volatile("s_waitcnt vmcnt(" #n ")" ::: "memory")

__global__ __launch_bounds__(256, 1) void gqa_kernel(const char* __restrict__ Q, const char* __restrict__ K, const char* __restrict__ V, char* __restrict__ O) {
  extern __shared__ __attribute__((aligned(16))) char smem[];
  const int tid = threadIdx.x, lane = tid & 63;
  const int w = __builtin_amdgcn_readfirstlane(tid >> 6);
  const int r = lane & 31, h = lane >> 5;
  const int bid = blockIdx.x, xcd = bid & 7, loc = bid >> 3;
  const int grp = xcd * 4 + (loc >> 3), j = loc & 7;
  const int qh = grp * 8 + j;
  const char* Kg = K + (size_t)grp * SK * 256;
  const char* Vg = V + (size_t)grp * SK * 256;
  const unsigned ldsb = (unsigned)(size_t)smem + w * (NSLOT * SLOT);
  unsigned ks[4];
#pragma unroll
  for (int g = 0; g < 4; ++g) ks[g] = (lane >> 4) * 256 + 16 * ((lane & 15) ^ ((lane >> 4) | (g << 2)));
  const unsigned vs = (lane >> 4) * 256 + 16 * ((lane & 15) ^ ((lane >> 4) << 2));
  const char* kp = Kg + (size_t)w * 8192 + 4096;
  const char* vp = Vg + (size_t)w * 8192 + 4096;
  constexpr int TSTEP = 4 * 8192;
  bf16x8 qf[8];
  {
    const char* qp = Q + ((size_t)qh * 32 + r) * 256 + h * 16;
    asm volatile(
      "global_load_dwordx4 %0, %8, off\n\t"
      "global_load_dwordx4 %1, %8, off offset:32\n\t"
      "global_load_dwordx4 %2, %8, off offset:64\n\t"
      "global_load_dwordx4 %3, %8, off offset:96\n\t"
      "global_load_dwordx4 %4, %8, off offset:128\n\t"
      "global_load_dwordx4 %5, %8, off offset:160\n\t"
      "global_load_dwordx4 %6, %8, off offset:192\n\t"
      "global_load_dwordx4 %7, %8, off offset:224\n\t"
      : "=&v"(qf[0]), "=&v"(qf[1]), "=&v"(qf[2]), "=&v"(qf[3]), "=&v"(qf[4]), "=&v"(qf[5]), "=&v"(qf[6]), "=&v"(qf[7]) : "v"(qp) : "memory");
  }
  dma_k(ldsb + 0 * SLOT + 4096, kp, ks[0], ks[1], ks[2], ks[3]);
  dma_v(ldsb + 1 * SLOT + 4096, vp, vs);
  dma_k(ldsb + 2 * SLOT + 4096, kp + TSTEP, ks[0], ks[1], ks[2], ks[3]);
  asm volatile("s_waitcnt vmcnt(24)" : "+v"(qf[0]), "+v"(qf[1]), "+v"(qf[2]), "+v"(qf[3]), "+v"(qf[4]), "+v"(qf[5]), "+v"(qf[6]), "+v"(qf[7]) :: "memory");
  const unsigned kf = (r & 3) | ((r >> 3) << 2);
  unsigned ka[8];
#pragma unroll
  for (int s = 0; s < 8; ++s) ka[s] = ldsb + r * 256 + 16 * ((2 * s + h) ^ kf);
  unsigned va[4];
  {
    const int blk = (lane >> 4) & 1, q4 = (lane & 15) >> 2, p = lane & 3;
#pragma unroll
    for (int c = 0; c < 4; ++c) va[c] = ldsb + (4 * h + q4) * 256 + 16 * ((4 * c + 2 * blk + (p >> 1)) ^ (q4 << 2)) + 8 * (p & 1);
  }
  f32x16 o[4] = {};
  float m = -1e30f, l = 0.f;

  auto tile = [&](int t, const int so  ) {
    if (t + 1 < TPW) WAITVM(16); else WAITVM(8);
    f32x16 s = {};
#pragma unroll
    for (int st = 0; st < 8; ++st) {
      bf16x8 a = *(const LAS bf16x8*)(ka[st] + so);
      s = __builtin_amdgcn_mfma_f32_32x32x16_bf16(a, qf[st], s, 0, 0, 0);
    }
    if (t + 1 < TPW) dma_v(ldsb + ((so + 3 * SLOT) & (4 * SLOT - 1)) + 4096, vp + (size_t)(t + 1) * TSTEP, vs);
    float mx = s[0];
#pragma unroll
    for (int i = 1; i < 16; ++i) mx = fmaxf(mx, s[i]);
    {
      auto rr = __builtin_amdgcn_permlane32_swap(__float_as_uint(mx), __float_as_uint(mx), false, false);
      mx = fmaxf(__uint_as_float(rr[0]), __uint_as_float(rr[1]));
    }
    mx *= CSC;
    if (__any(mx > m)) {
      float mn = fmaxf(m, mx);
      float alpha = __builtin_amdgcn_exp2f(m - mn);
      m = mn;
      l *= alpha;
#pragma unroll
      for (int db = 0; db < 4; ++db)
#pragma unroll
        for (int i = 0; i < 16; ++i) o[db][i] *= alpha;
    }
    float ps = 0.f;
#pragma unroll
    for (int i = 0; i < 16; ++i) { s[i] = __builtin_amdgcn_exp2f(fmaf(s[i], CSC, -m)); ps += s[i]; }
    l += ps;
    bf16x8 pf[2];
#pragma unroll
    for (int k2 = 0; k2 < 2; ++k2) {
      u32x4 u;
#pragma unroll
      for (int e = 0; e < 4; ++e) u[e] = cvtpk(s[8 * k2 + 2 * e], s[8 * k2 + 2 * e + 1]);
      pf[k2] = __builtin_bit_cast(bf16x8, u);
    }
    if (t + 1 < TPW) WAITVM(16); else WAITVM(0);
#pragma unroll
    for (int db = 0; db < 4; ++db)
#pragma unroll
      for (int k2 = 0; k2 < 2; ++k2) {
        s16x4 lo = __builtin_amdgcn_ds_read_tr16_b64_v4i16((LAS s16x4*)(va[db] + so + SLOT + k2 * 4096));
        s16x4 hi = __builtin_amdgcn_ds_read_tr16_b64_v4i16((LAS s16x4*)(va[db] + so + SLOT + k2 * 4096 + 2048));
        bf16x8 a = __builtin_bit_cast(bf16x8, __builtin_shufflevector(lo, hi, 0, 1, 2, 3, 4, 5, 6, 7));
        o[db] = __builtin_amdgcn_mfma_f32_32x32x16_bf16(a, pf[k2], o[db], 0, 0, 0);
      }
    if (t + 2 < TPW) dma_k(ldsb + so + 4096, kp + (size_t)(t + 2) * TSTEP, ks[0], ks[1], ks[2], ks[3]);
  };
  for (int t = 0; t < TPW; t += 2) {
    tile(t, 0);
    tile(t + 1, 2 * SLOT);
  }
  {
    auto rr = __builtin_amdgcn_permlane32_swap(__float_as_uint(l), __float_as_uint(l), false, false);
    l = __uint_as_float(rr[0]) + __uint_as_float(rr[1]);
  }
  __syncthreads();
  float* sm_m = (float*)smem;
  float* sm_l = sm_m + 128;
  float* sm_o = (float*)(smem + 1024);
  if (h == 0) { sm_m[w * 32 + r] = m; sm_l[w * 32 + r] = l; }
  __syncthreads();
  float M = fmaxf(fmaxf(sm_m[r], sm_m[32 + r]), fmaxf(sm_m[64 + r], sm_m[96 + r]));
  float L = 0.f;
#pragma unroll
  for (int i = 0; i < 4; ++i) L += sm_l[i * 32 + r] * __builtin_amdgcn_exp2f(sm_m[i * 32 + r] - M);
  const float sc = __builtin_amdgcn_exp2f(m - M) / L;
#pragma unroll
  for (int db = 0; db < 4; ++db)
#pragma unroll
    for (int g = 0; g < 4; ++g) {
      f32x4 x = {o[db][4 * g] * sc, o[db][4 * g + 1] * sc, o[db][4 * g + 2] * sc, o[db][4 * g + 3] * sc};
      *(f32x4*)(sm_o + (((w * 4 + db) * 4 + g) * 64 + lane) * 4) = x;
    }
  __syncthreads();
  char* op = O + ((size_t)qh * 32 + r) * 256 + w * 64 + h * 8;
#pragma unroll
  for (int g = 0; g < 4; ++g) {
    f32x4 x = *(f32x4*)(sm_o + (((0 * 4 + w) * 4 + g) * 64 + lane) * 4);
#pragma unroll
    for (int i = 1; i < 4; ++i) x += *(f32x4*)(sm_o + (((i * 4 + w) * 4 + g) * 64 + lane) * 4);
    u32x2 u = {cvtpk(x[0], x[1]), cvtpk(x[2], x[3])};
    *(u32x2*)(op + g * 16) = u;
  }
}

extern "C" void kernel_launch(void* const* d_in, const int* in_sizes, int n_in, void* d_out, int out_size, void* d_ws, size_t ws_size, hipStream_t stream) {
  constexpr int SHM = NW * NSLOT * SLOT;
  (void)hipFuncSetAttribute((const void*)gqa_kernel, hipFuncAttributeMaxDynamicSharedMemorySize, SHM);
  hipLaunchKernelGGL(gqa_kernel, dim3(256), dim3(256), SHM, stream, (const char*)d_in[0], (const char*)d_in[1], (const char*)d_in[2], (char*)d_out);
}
```

```cpp
#include <hip/hip_runtime.h>
#include <hip/hip_bf16.h>

typedef float f32x16 __attribute__((ext_vector_type(16)));
typedef float f32x2 __attribute__((ext_vector_type(2)));
typedef __bf16 bf16x2 __attribute__((ext_vector_type(2)));
typedef unsigned u32x4 __attribute__((ext_vector_type(4)));
typedef unsigned u32x2 __attribute__((ext_vector_type(2)));
typedef __amdgpu_buffer_rsrc_t srd_t;
#define DI __device__ __forceinline__

constexpr float CSC = 0.088388347648318447f * 1.4426950408889634f;
constexpr float PS_LIMIT = 1024.f;
constexpr unsigned LDS_K = 0x8000, LDS_BYTES = 0x28000;
constexpr unsigned WS_HEAD = 28672, WS_INBOX = 7168, WS_RDY = 6912;

DI unsigned cvtpk(float lo, float hi) { f32x2 v = {lo, hi}; bf16x2 b = __builtin_convertvector(v, bf16x2); return __builtin_bit_cast(unsigned, b); }
#define SPIN_MAX (1u << 20)
DI srd_t mk_srd(const char* p, unsigned n) { return __builtin_amdgcn_make_buffer_rsrc((void*)p, (short)0, (int)n, 0x00020000); }
#define SETM0(x) asm volatile("s_mov_b32 m0, %0\n\ts_nop 0" :: "s"(x) : "memory")
#define DMA(voff, srd, soff, OFF) asm volatile("buffer_load_dwordx4 %0, %1, %2 offen offset:" #OFF "  lds" :: "v"(voff), "s"(srd), "s"(soff) : "memory")

__global__ __launch_bounds__(256, 1) void gqa_kernel(const char* __restrict__ Q, const char* __restrict__ K, const char* __restrict__ V, char* __restrict__ O, char* ws) {
  extern __shared__ __attribute__((aligned(16))) char smem[];
  unsigned j0;
  unsigned long long pc;
  const unsigned pfo = min((blockIdx.x >> 3) * 768u + min(threadIdx.x >> 4, 11u) * 64u, 18432u);
  asm volatile("s_getpc_b64 %1\n\tglobal_load_dword %0, %2, %1" : "={v245}"(j0), "=&s"(pc) : "{v237}"(pfo) : "memory");
  __builtin_amdgcn_sched_barrier(0);
  const unsigned tid = threadIdx.x, lane = tid & 63u;
  const unsigned w = __builtin_amdgcn_readfirstlane(tid >> 6);
  const unsigned c16 = lane & 15u, g4 = lane >> 4, h = lane >> 5;
  const unsigned bid = blockIdx.x, xcd = bid & 7u, loc = bid >> 3;
  const unsigned grp = xcd * 4u + (loc >> 3), sid = loc & 1u, sp = (loc >> 1) & 3u;
  const unsigned qh = grp * 8u + sid * 4u + w;
  const bool odd = w >= 2u;
  const unsigned hw4 = (w & 1u) * 4096u;
  const size_t kvoff = (size_t)(grp * 2048u + sp * 512u) * 256u + hw4;
  const unsigned drow = (lane >> 2) & 7u;
  const unsigned qa = drow * 256u + h * 64u + ((lane & 3u) ^ ((g4 & 1u) * 3u)) * 16u + 1024u;
  const unsigned qb = qa ^ 32u, qc = qa + 128u - 1024u, qd = qb + 128u - 1024u;
  const unsigned ts = sid * 8192u;
  {
    const srd_t sQ = mk_srd(Q + (size_t)qh * 8192u - 1024, 1024u + 8192u);
    asm volatile("s_nop 4" ::: "memory");
    SETM0(w * 8192u);
    DMA(qa, sQ, 0, 0); DMA(qb, sQ, 0, 2048);
    SETM0(w * 8192u + 4096u);
    DMA(qa, sQ, 4096, 0); DMA(qb, sQ, 4096, 2048);
    const srd_t sK = mk_srd(K + (kvoff - hw4) + w * 2048u - 1024, 1024u + 16u * 8192u - w * 2048u);
    const unsigned kb = (w & 1u) ? qb : qa, kc = (w & 1u) ? qd : qc;
    SETM0(LDS_K + w * 2048u);
    DMA(kb, sK, ts, 0); DMA(kc, sK, ts, 1024);
    SETM0(w * 8192u);
    DMA(qc, sQ, 0, 1024); DMA(qd, sQ, 0, 3072);
    SETM0(w * 8192u + 4096u);
    DMA(qc, sQ, 4096, 1024); DMA(qd, sQ, 4096, 3072);
    SETM0(LDS_K + 8192u + w * 2048u);
    DMA(kb, sK, 15u * 8192u - ts, 0); DMA(kc, sK, 15u * 8192u - ts, 1024);
  }
  char* pay = ws + (size_t)qh * WS_HEAD;
  const unsigned px = (lane & 3u) ^ sp;
  const unsigned faddr = sp * WS_INBOX + WS_RDY + (lane & 3u) * 4u, waddr = px * WS_INBOX + WS_RDY + (lane & 3u) * 4u;
  const unsigned v0 = drow * 256u + ((h ^ sp) & 1u) * 64u + ((lane & 3u) ^ ((g4 & 1u) * 2u)) * 16u;
  const int sh = odd ? -8192 - (int)ts : (int)ts;
  const srd_t sKs = mk_srd(K + kvoff - 1024 + sh, 1024u + (16u * 8192u - hw4 - sh));
  const int shv = (int)(w * 8192u) + ((w & 1u) ? -16384 - (int)ts : (int)ts), lo = (int)(sp & 2u) * 64;
  const srd_t sVL = mk_srd(V + (kvoff - hw4) + shv + lo, (16u * 8192u - shv - lo));
  const srd_t sVH = mk_srd(V + (kvoff - hw4) + shv + (128 - lo), (16u * 8192u - shv - (128 - lo)));
  const unsigned fr = (c16 >> 3) * 2048u + (c16 & 7u) * 64u + 16u * (g4 ^ ((0u - (c16 >> 2)) & 3u));
  unsigned ka = LDS_K + fr;
  const unsigned va = 1024u * h + 64u * (4u * (g4 & 1u) + (c16 >> 2)) + 32u * (g4 & 1u) + 8u * (lane & 3u);
  unsigned qra = w * 8192u + fr;
  f32x16 o0; u32x4 ml, s1, s2, s3, m1a, m1b, m2a, m2b, m3a, m3b, x1a, x1b, x2a, x2b, x3a, x3b; unsigned rdy;
  const char* inb = pay + sp * WS_INBOX + 3072u;
  char* dp1 = pay + (1u ^ sp) * WS_INBOX + 3072u;
  char* dp2 = pay + (2u ^ sp) * WS_INBOX + 3072u;
  char* dp3 = pay + (3u ^ sp) * WS_INBOX + 3072u;
  asm volatile(
      "s_waitcnt vmcnt(8)\n\t"
      "ds_read_b128 v[0:3], v228 offset:0\n\t"
      "ds_read_b128 v[16:19], v228 offset:4096\n\t"
      "ds_read_b128 v[4:7], v228 offset:512\n\t"
      "ds_read_b128 v[20:23], v228 offset:4608\n\t"
      "s_add_u32 m0, %[sMW], 49152\n\t"
      "s_mov_b32 s40, 16384\n\t"
      "buffer_load_dwordx4 v224, %[sK], s40 offen offset:0 lds\n\t"
      "buffer_load_dwordx4 v225, %[sK], s40 offen offset:1024 lds\n\t"
      "buffer_load_dwordx4 v226, %[sK], s40 offen offset:2048 lds\n\t"
      "buffer_load_dwordx4 v227, %[sK], s40 offen offset:3072 lds\n\t"
      "s_waitcnt lgkmcnt(0)\n\t"
      "v_lshlrev_b32 v208, 16, v0\n\t"
      "v_and_b32 v209, 0xffff0000, v0\n\t"
      "v_mul_f32 v208, %[sCSC], v208\n\t"
      "v_mul_f32 v209, %[sCSC], v209\n\t"
      "v_cvt_pk_bf16_f32 v0, v208, v209\n\t"
      "v_lshlrev_b32 v210, 16, v1\n\t"
      "v_and_b32 v211, 0xffff0000, v1\n\t"
      "v_mul_f32 v210, %[sCSC], v210\n\t"
      "v_mul_f32 v211, %[sCSC], v211\n\t"
      "v_cvt_pk_bf16_f32 v1, v210, v211\n\t"
      "v_lshlrev_b32 v212, 16, v2\n\t"
      "v_and_b32 v213, 0xffff0000, v2\n\t"
      "v_mul_f32 v212, %[sCSC], v212\n\t"
      "v_mul_f32 v213, %[sCSC], v213\n\t"
      "v_cvt_pk_bf16_f32 v2, v212, v213\n\t"
      "v_lshlrev_b32 v214, 16, v3\n\t"
      "v_and_b32 v215, 0xffff0000, v3\n\t"
      "v_mul_f32 v214, %[sCSC], v214\n\t"
      "v_mul_f32 v215, %[sCSC], v215\n\t"
      "v_cvt_pk_bf16_f32 v3, v214, v215\n\t"
      "v_lshlrev_b32 v208, 16, v16\n\t"
      "v_and_b32 v209, 0xffff0000, v16\n\t"
      "v_mul_f32 v208, %[sCSC], v208\n\t"
      "v_mul_f32 v209, %[sCSC], v209\n\t"
      "v_cvt_pk_bf16_f32 v16, v208, v209\n\t"
      "v_lshlrev_b32 v210, 16, v17\n\t"
      "v_and_b32 v211, 0xffff0000, v17\n\t"
      "v_mul_f32 v210, %[sCSC], v210\n\t"
      "v_mul_f32 v211, %[sCSC], v211\n\t"
      "v_cvt_pk_bf16_f32 v17, v210, v211\n\t"
      "v_lshlrev_b32 v212, 16, v18\n\t"
      "v_and_b32 v213, 0xffff0000, v18\n\t"
      "v_mul_f32 v212, %[sCSC], v212\n\t"
      "v_mul_f32 v213, %[sCSC], v213\n\t"
      "v_cvt_pk_bf16_f32 v18, v212, v213\n\t"
      "v_lshlrev_b32 v214, 16, v19\n\t"
      "v_and_b32 v215, 0xffff0000, v19\n\t"
      "v_mul_f32 v214, %[sCSC], v214\n\t"
      "v_mul_f32 v215, %[sCSC], v215\n\t"
      "v_cvt_pk_bf16_f32 v19, v214, v215\n\t"
      "s_waitcnt vmcnt(10)\n\t"
      "s_barrier\n\t"
      "ds_read_b128 a[96:99], v220 offset:0\n\t"
      "ds_read_b128 a[100:103], v220 offset:4096\n\t"
      "ds_read_b128 a[104:107], v220 offset:512\n\t"
      "ds_read_b128 a[108:111], v220 offset:4608\n\t"
      "ds_read_b128 a[112:115], v220 offset:1024\n\t"
      "ds_read_b128 a[116:119], v220 offset:5120\n\t"
      "ds_read_b128 a[120:123], v220 offset:1536\n\t"
      "ds_read_b128 a[124:127], v220 offset:5632\n\t"
      "v_lshlrev_b32 v208, 16, v4\n\t"
      "v_and_b32 v209, 0xffff0000, v4\n\t"
      "v_mul_f32 v208, %[sCSC], v208\n\t"
      "v_mul_f32 v209, %[sCSC], v209\n\t"
      "v_cvt_pk_bf16_f32 v4, v208, v209\n\t"
      "v_lshlrev_b32 v210, 16, v5\n\t"
      "v_and_b32 v211, 0xffff0000, v5\n\t"
      "v_mul_f32 v210, %[sCSC], v210\n\t"
      "v_mul_f32 v211, %[sCSC], v211\n\t"
      "v_cvt_pk_bf16_f32 v5, v210, v211\n\t"
      "v_lshlrev_b32 v212, 16, v6\n\t"
      "v_and_b32 v213, 0xffff0000, v6\n\t"
      "v_mul_f32 v212, %[sCSC], v212\n\t"
      "v_mul_f32 v213, %[sCSC], v213\n\t"
      "v_cvt_pk_bf16_f32 v6, v212, v213\n\t"
      "v_lshlrev_b32 v214, 16, v7\n\t"
      "v_and_b32 v215, 0xffff0000, v7\n\t"
      "v_mul_f32 v214, %[sCSC], v214\n\t"
      "v_mul_f32 v215, %[sCSC], v215\n\t"
      "v_cvt_pk_bf16_f32 v7, v214, v215\n\t"
      "s_waitcnt lgkmcnt(4)\n\t"
      "v_mfma_f32_16x16x32_bf16 v[32:35], a[96:99], v[0:3], 0\n\t"
      "v_mfma_f32_16x16x32_bf16 v[36:39], a[100:103], v[0:3], 0\n\t"
      "v_lshlrev_b32 v208, 16, v20\n\t"
      "v_and_b32 v209, 0xffff0000, v20\n\t"
      "v_mul_f32 v208, %[sCSC], v208\n\t"
      "v_mul_f32 v209, %[sCSC], v209\n\t"
      "v_cvt_pk_bf16_f32 v20, v208, v209\n\t"
      "v_lshlrev_b32 v210, 16, v21\n\t"
      "v_and_b32 v211, 0xffff0000, v21\n\t"
      "v_mul_f32 v210, %[sCSC], v210\n\t"
      "v_mul_f32 v211, %[sCSC], v211\n\t"
      "v_cvt_pk_bf16_f32 v21, v210, v211\n\t"
      "v_lshlrev_b32 v212, 16, v22\n\t"
      "v_and_b32 v213, 0xffff0000, v22\n\t"
      "v_mul_f32 v212, %[sCSC], v212\n\t"
      "v_mul_f32 v213, %[sCSC], v213\n\t"
      "v_cvt_pk_bf16_f32 v22, v212, v213\n\t"
      "v_lshlrev_b32 v214, 16, v23\n\t"
      "v_and_b32 v215, 0xffff0000, v23\n\t"
      "v_mul_f32 v214, %[sCSC], v214\n\t"
      "v_mul_f32 v215, %[sCSC], v215\n\t"
      "v_cvt_pk_bf16_f32 v23, v214, v215\n\t"
      "v_mfma_f32_16x16x32_bf16 v[40:43], a[96:99], v[16:19], 0\n\t"
      "v_mfma_f32_16x16x32_bf16 v[44:47], a[100:103], v[16:19], 0\n\t"
      "s_waitcnt vmcnt(6)\n\t"
      "ds_read_b128 v[8:11], v228 offset:1024\n\t"
      "ds_read_b128 v[24:27], v228 offset:5120\n\t"
      "ds_read_b128 v[12:15], v228 offset:1536\n\t"
      "ds_read_b128 v[28:31], v228 offset:5632\n\t"
      "s_add_u32 m0, %[sMW], 65536\n\t"
      "s_mov_b32 s40, 32768\n\t"
      "buffer_load_dwordx4 v224, %[sK], s40 offen offset:0 lds\n\t"
      "buffer_load_dwordx4 v225, %[sK], s40 offen offset:1024 lds\n\t"
      "buffer_load_dwordx4 v226, %[sK], s40 offen offset:2048 lds\n\t"
      "buffer_load_dwordx4 v227, %[sK], s40 offen offset:3072 lds\n\t"
      "v_mfma_f32_16x16x32_bf16 v[32:35], a[104:107], v[4:7], v[32:35]\n\t"
      "v_mfma_f32_16x16x32_bf16 v[36:39], a[108:111], v[4:7], v[36:39]\n\t"
      "s_waitcnt lgkmcnt(3)\n\t"
      "v_lshlrev_b32 v208, 16, v8\n\t"
      "v_and_b32 v209, 0xffff0000, v8\n\t"
      "v_mul_f32 v208, %[sCSC], v208\n\t"
      "v_mul_f32 v209, %[sCSC], v209\n\t"
      "v_cvt_pk_bf16_f32 v8, v208, v209\n\t"
      "v_lshlrev_b32 v210, 16, v9\n\t"
      "v_and_b32 v211, 0xffff0000, v9\n\t"
      "v_mul_f32 v210, %[sCSC], v210\n\t"
      "v_mul_f32 v211, %[sCSC], v211\n\t"
      "v_cvt_pk_bf16_f32 v9, v210, v211\n\t"
      "v_lshlrev_b32 v212, 16, v10\n\t"
      "v_and_b32 v213, 0xffff0000, v10\n\t"
      "v_mul_f32 v212, %[sCSC], v212\n\t"
      "v_mul_f32 v213, %[sCSC], v213\n\t"
      "v_cvt_pk_bf16_f32 v10, v212, v213\n\t"
      "v_lshlrev_b32 v214, 16, v11\n\t"
      "v_and_b32 v215, 0xffff0000, v11\n\t"
      "v_mul_f32 v214, %[sCSC], v214\n\t"
      "v_mul_f32 v215, %[sCSC], v215\n\t"
      "v_cvt_pk_bf16_f32 v11, v214, v215\n\t"
      "v_mfma_f32_16x16x32_bf16 v[40:43], a[104:107], v[20:23], v[40:43]\n\t"
      "v_mfma_f32_16x16x32_bf16 v[44:47], a[108:111], v[20:23], v[44:47]\n\t"
      "s_waitcnt lgkmcnt(0)\n\t"
      "v_lshlrev_b32 v208, 16, v24\n\t"
      "v_and_b32 v209, 0xffff0000, v24\n\t"
      "v_mul_f32 v208, %[sCSC], v208\n\t"
      "v_mul_f32 v209, %[sCSC], v209\n\t"
      "v_cvt_pk_bf16_f32 v24, v208, v209\n\t"
      "v_lshlrev_b32 v210, 16, v25\n\t"
      "v_and_b32 v211, 0xffff0000, v25\n\t"
      "v_mul_f32 v210, %[sCSC], v210\n\t"
      "v_mul_f32 v211, %[sCSC], v211\n\t"
      "v_cvt_pk_bf16_f32 v25, v210, v211\n\t"
      "v_lshlrev_b32 v212, 16, v26\n\t"
      "v_and_b32 v213, 0xffff0000, v26\n\t"
      "v_mul_f32 v212, %[sCSC], v212\n\t"
      "v_mul_f32 v213, %[sCSC], v213\n\t"
      "v_cvt_pk_bf16_f32 v26, v212, v213\n\t"
      "v_lshlrev_b32 v214, 16, v27\n\t"
      "v_and_b32 v215, 0xffff0000, v27\n\t"
      "v_mul_f32 v214, %[sCSC], v214\n\t"
      "v_mul_f32 v215, %[sCSC], v215\n\t"
      "v_cvt_pk_bf16_f32 v27, v214, v215\n\t"
      "v_mfma_f32_16x16x32_bf16 v[32:35], a[112:115], v[8:11], v[32:35]\n\t"
      "v_mfma_f32_16x16x32_bf16 v[36:39], a[116:119], v[8:11], v[36:39]\n\t"
      "v_lshlrev_b32 v208, 16, v12\n\t"
      "v_and_b32 v209, 0xffff0000, v12\n\t"
      "v_mul_f32 v208, %[sCSC], v208\n\t"
      "v_mul_f32 v209, %[sCSC], v209\n\t"
      "v_cvt_pk_bf16_f32 v12, v208, v209\n\t"
      "v_lshlrev_b32 v210, 16, v13\n\t"
      "v_and_b32 v211, 0xffff0000, v13\n\t"
      "v_mul_f32 v210, %[sCSC], v210\n\t"
      "v_mul_f32 v211, %[sCSC], v211\n\t"
      "v_cvt_pk_bf16_f32 v13, v210, v211\n\t"
      "v_lshlrev_b32 v212, 16, v14\n\t"
      "v_and_b32 v213, 0xffff0000, v14\n\t"
      "v_mul_f32 v212, %[sCSC], v212\n\t"
      "v_mul_f32 v213, %[sCSC], v213\n\t"
      "v_cvt_pk_bf16_f32 v14, v212, v213\n\t"
      "v_lshlrev_b32 v214, 16, v15\n\t"
      "v_and_b32 v215, 0xffff0000, v15\n\t"
      "v_mul_f32 v214, %[sCSC], v214\n\t"
      "v_mul_f32 v215, %[sCSC], v215\n\t"
      "v_cvt_pk_bf16_f32 v15, v214, v215\n\t"
      "v_mfma_f32_16x16x32_bf16 v[40:43], a[112:115], v[24:27], v[40:43]\n\t"
      "v_mfma_f32_16x16x32_bf16 v[44:47], a[116:119], v[24:27], v[44:47]\n\t"
      "s_waitcnt vmcnt(8)\n\t"
      "s_barrier\n\t"
      "s_add_u32 m0, %[sMW], 0\n\t"
      "s_nop 0\n\t"
      "buffer_load_dwordx4 v239, %[sVH], %[sV01] offen offset:0 lds\n\t"
      "buffer_load_dwordx4 v235, %[sVH], %[sV01] offen offset:1024 lds\n\t"
      "buffer_load_dwordx4 v246, %[sVH], %[sV01] offen offset:2048 lds\n\t"
      "buffer_load_dwordx4 v247, %[sVH], %[sV01] offen offset:3072 lds\n\t"
      "v_lshlrev_b32 v208, 16, v28\n\t"
      "v_and_b32 v209, 0xffff0000, v28\n\t"
      "v_mul_f32 v208, %[sCSC], v208\n\t"
      "v_mul_f32 v209, %[sCSC], v209\n\t"
      "v_cvt_pk_bf16_f32 v28, v208, v209\n\t"
      "v_lshlrev_b32 v210, 16, v29\n\t"
      "v_and_b32 v211, 0xffff0000, v29\n\t"
      "v_mul_f32 v210, %[sCSC], v210\n\t"
      "v_mul_f32 v211, %[sCSC], v211\n\t"
      "v_cvt_pk_bf16_f32 v29, v210, v211\n\t"
      "v_lshlrev_b32 v212, 16, v30\n\t"
      "v_and_b32 v213, 0xffff0000, v30\n\t"
      "v_mul_f32 v212, %[sCSC], v212\n\t"
      "v_mul_f32 v213, %[sCSC], v213\n\t"
      "v_cvt_pk_bf16_f32 v30, v212, v213\n\t"
      "v_lshlrev_b32 v214, 16, v31\n\t"
      "v_and_b32 v215, 0xffff0000, v31\n\t"
      "v_mul_f32 v214, %[sCSC], v214\n\t"
      "v_mul_f32 v215, %[sCSC], v215\n\t"
      "v_cvt_pk_bf16_f32 v31, v214, v215\n\t"
      "v_mfma_f32_16x16x32_bf16 v[32:35], a[120:123], v[12:15], v[32:35]\n\t"
      "v_mfma_f32_16x16x32_bf16 v[36:39], a[124:127], v[12:15], v[36:39]\n\t"
      "v_mfma_f32_16x16x32_bf16 v[40:43], a[120:123], v[28:31], v[40:43]\n\t"
      "v_mfma_f32_16x16x32_bf16 v[44:47], a[124:127], v[28:31], v[44:47]\n\t"
      "ds_read_b128 a[64:67], v220 offset:8192\n\t"
      "ds_read_b128 a[68:71], v220 offset:8704\n\t"
      "ds_read_b128 a[72:75], v220 offset:9216\n\t"
      "ds_read_b128 a[76:79], v220 offset:9728\n\t"
      "ds_read_b128 a[80:83], v220 offset:12288\n\t"
      "ds_read_b128 a[84:87], v220 offset:12800\n\t"
      "ds_read_b128 a[88:91], v220 offset:13312\n\t"
      "v_mov_b32 v200, -1\n\t"
      "v_mov_b32 v201, -1\n\t"
      "v_mov_b32 v202, -1\n\t"
      "v_mov_b32 v203, -1\n\t"
      "v_mov_b32 v230, 1\n\t"
      "v_max3_f32 v228, v32, v33, v34\n\t"
      "v_max3_f32 v229, v40, v41, v42\n\t"
      "v_max3_f32 v228, v228, v35, v36\n\t"
      "v_max3_f32 v229, v229, v43, v44\n\t"
      "v_max3_f32 v228, v228, v37, v38\n\t"
      "v_max3_f32 v229, v229, v45, v46\n\t"
      "v_max_f32 v228, v228, v39\n\t"
      "v_max_f32 v229, v229, v47\n\t"
      "v_mov_b32 v75, v228\n\t"
      "v_mov_b32 v76, v229\n\t"
      "s_nop 0\n\t"
      "v_permlane32_swap_b32 v228, v75\n\t"
      "v_permlane32_swap_b32 v229, v76\n\t"
      "s_nop 0\n\t"
      "v_max_f32 v228, v228, v75\n\t"
      "v_max_f32 v229, v229, v76\n\t"
      "v_mov_b32 v75, v228\n\t"
      "v_mov_b32 v76, v229\n\t"
      "s_nop 0\n\t"
      "v_permlane16_swap_b32 v228, v75\n\t"
      "v_permlane16_swap_b32 v229, v76\n\t"
      "s_nop 0\n\t"
      "v_max_f32 v228, v228, v75\n\t"
      "v_max_f32 v229, v229, v76\n\t"
      "v_ceil_f32 v228, v228\n\t"
      "v_ceil_f32 v229, v229\n\t"
      "v_sub_f32 v64, 0, v228\n\t"
      "v_sub_f32 v65, 0, v228\n\t"
      "v_sub_f32 v66, 0, v228\n\t"
      "v_sub_f32 v67, 0, v228\n\t"
      "v_sub_f32 v68, 0, v229\n\t"
      "v_sub_f32 v69, 0, v229\n\t"
      "v_sub_f32 v70, 0, v229\n\t"
      "v_sub_f32 v71, 0, v229\n\t"
      "v_sub_f32 v32, v32, v228\n\t"
      "v_sub_f32 v33, v33, v228\n\t"
      "v_sub_f32 v34, v34, v228\n\t"
      "v_sub_f32 v35, v35, v228\n\t"
      "v_sub_f32 v36, v36, v228\n\t"
      "v_sub_f32 v37, v37, v228\n\t"
      "v_sub_f32 v38, v38, v228\n\t"
      "v_sub_f32 v39, v39, v228\n\t"
      "v_sub_f32 v40, v40, v229\n\t"
      "v_sub_f32 v41, v41, v229\n\t"
      "v_sub_f32 v42, v42, v229\n\t"
      "v_sub_f32 v43, v43, v229\n\t"
      "v_sub_f32 v44, v44, v229\n\t"
      "v_sub_f32 v45, v45, v229\n\t"
      "v_sub_f32 v46, v46, v229\n\t"
      "v_sub_f32 v47, v47, v229\n\t"
      "s_waitcnt lgkmcnt(6)\n\t"
      "v_mfma_f32_16x16x32_bf16 v[48:51], a[64:67], v[0:3], v[64:67]\n\t"
      "ds_read_b128 a[92:95], v220 offset:13824\n\t"
      "v_exp_f32 v216, v32\n\t"
      "v_mfma_f32_16x16x32_bf16 v[56:59], a[64:67], v[16:19], v[68:71]\n\t"
      "global_store_dwordx4 v233, v[200:203], %[sIN] offset:-3072 sc1\n\t"
      "s_waitcnt lgkmcnt(4)\n\t"
      "v_exp_f32 v209, v33\n\t"
      "v_exp_f32 v210, v34\n\t"
      "v_cvt_pk_bf16_f32 v80, v216, v209\n\t"
      "s_nop 5\n\t"
      "v_mfma_f32_16x16x32_bf16 v[48:51], a[68:71], v[4:7], v[48:51]\n\t"
      "v_mfma_f32_16x16x32_bf16 v[56:59], a[68:71], v[20:23], v[56:59]\n\t"
      "s_waitcnt vmcnt(9)\n\t"
      "s_barrier\n\t"
      "ds_read_b128 a[96:99], v220 offset:16384\n\t"
      "s_add_u32 m0, %[sMW], 81920\n\t"
      "s_mov_b32 s40, 49152\n\t"
      "v_exp_f32 v211, v35\n\t"
      "v_add_f32 v216, v216, v209\n\t"
      "s_nop 5\n\t"
      "v_mfma_f32_16x16x32_bf16 v[48:51], a[72:75], v[8:11], v[48:51]\n\t"
      "ds_read_b128 a[100:103], v220 offset:16896\n\t"
      "v_exp_f32 v212, v36\n\t"
      "v_mfma_f32_16x16x32_bf16 v[56:59], a[72:75], v[24:27], v[56:59]\n\t"
      "v_cvt_pk_bf16_f32 v81, v210, v211\n\t"
      "v_add_f32 v216, v216, v210\n\t"
      "v_exp_f32 v213, v37\n\t"
      "v_add_f32 v216, v216, v211\n\t"
      "v_exp_f32 v214, v38\n\t"
      "v_cvt_pk_bf16_f32 v82, v212, v213\n\t"
      "s_nop 4\n\t"
      "v_mfma_f32_16x16x32_bf16 v[48:51], a[76:79], v[12:15], v[48:51]\n\t"
      "ds_read_b128 a[104:107], v220 offset:17408\n\t"
      "v_add_f32 v216, v216, v212\n\t"
      "v_mfma_f32_16x16x32_bf16 v[56:59], a[76:79], v[28:31], v[56:59]\n\t"
      "buffer_load_dwordx4 v224, %[sK], s40 offen offset:0 lds\n\t"
      "s_waitcnt lgkmcnt(4)\n\t"
      "v_exp_f32 v215, v39\n\t"
      "v_add_f32 v216, v216, v213\n\t"
      "v_exp_f32 v217, v40\n\t"
      "v_cvt_pk_bf16_f32 v83, v214, v215\n\t"
      "s_nop 5\n\t"
      "v_mfma_f32_16x16x32_bf16 v[52:55], a[80:83], v[0:3], v[64:67]\n\t"
      "ds_read_b128 a[108:111], v220 offset:17920\n\t"
      "v_add_f32 v216, v216, v214\n\t"
      "v_mfma_f32_16x16x32_bf16 v[60:63], a[80:83], v[16:19], v[68:71]\n\t"
      "buffer_load_dwordx4 v225, %[sK], s40 offen offset:1024 lds\n\t"
      "v_exp_f32 v209, v41\n\t"
      "v_add_f32 v216, v216, v215\n\t"
      "v_exp_f32 v210, v42\n\t"
      "v_cvt_pk_bf16_f32 v84, v217, v209\n\t"
      "s_nop 6\n\t"
      "v_mfma_f32_16x16x32_bf16 v[52:55], a[84:87], v[4:7], v[52:55]\n\t"
      "ds_read_b128 a[112:115], v220 offset:20480\n\t"
      "v_exp_f32 v211, v43\n\t"
      "v_mfma_f32_16x16x32_bf16 v[60:63], a[84:87], v[20:23], v[60:63]\n\t"
      "v_add_f32 v217, v217, v209\n\t"
      "v_exp_f32 v212, v44\n\t"
      "v_cvt_pk_bf16_f32 v85, v210, v211\n\t"
      "v_add_f32 v217, v217, v210\n\t"
      "v_exp_f32 v213, v45\n\t"
      "s_nop 5\n\t"
      "v_mfma_f32_16x16x32_bf16 v[52:55], a[88:91], v[8:11], v[52:55]\n\t"
      "ds_read_b128 a[116:119], v220 offset:20992\n\t"
      "v_add_f32 v217, v217, v211\n\t"
      "v_mfma_f32_16x16x32_bf16 v[60:63], a[88:91], v[24:27], v[60:63]\n\t"
      "buffer_load_dwordx4 v226, %[sK], s40 offen offset:2048 lds\n\t"
      "s_waitcnt lgkmcnt(4)\n\t"
      "v_exp_f32 v214, v46\n\t"
      "v_cvt_pk_bf16_f32 v86, v212, v213\n\t"
      "v_add_f32 v217, v217, v212\n\t"
      "v_exp_f32 v215, v47\n\t"
      "s_nop 5\n\t"
      "v_mfma_f32_16x16x32_bf16 v[52:55], a[92:95], v[12:15], v[52:55]\n\t"
      "ds_read_b128 a[120:123], v220 offset:21504\n\t"
      "v_add_f32 v217, v217, v213\n\t"
      "v_mfma_f32_16x16x32_bf16 v[60:63], a[92:95], v[28:31], v[60:63]\n\t"
      "v_add_f32 v217, v217, v214\n\t"
      "v_cvt_pk_bf16_f32 v87, v214, v215\n\t"
      "v_add_f32 v217, v217, v215\n\t"
      "v_add_f32 v72, v216, v217\n\t"
      "v_cmp_nge_f32 vcc, %[sLIM], v72\n\t"
      "s_cbranch_vccnz .Lr0_%=\n\t"
      ".Lb0_%=:\n\t"
      "v_mov_b32 v218, v216\n\t"
      "v_mov_b32 v219, v217\n\t"
      "s_nop 5\n\t"
      "v_mfma_f32_16x16x32_bf16 v[32:35], a[96:99], v[0:3], v[64:67]\n\t"
      "ds_read_b128 a[124:127], v220 offset:22016\n\t"
      "v_exp_f32 v216, v48\n\t"
      "v_mfma_f32_16x16x32_bf16 v[40:43], a[96:99], v[16:19], v[68:71]\n\t"
      "buffer_load_dwordx4 v227, %[sK], s40 offen offset:3072 lds\n\t"
      "global_store_dwordx4 v233, v[200:203], %[sIN] offset:-2048 sc1\n\t"
      "v_exp_f32 v209, v49\n\t"
      "v_exp_f32 v210, v50\n\t"
      "v_cvt_pk_bf16_f32 v88, v216, v209\n\t"
      "s_nop 5\n\t"
      "v_mfma_f32_16x16x32_bf16 v[32:35], a[100:103], v[4:7], v[32:35]\n\t"
      "ds_read_b128 a[64:67], v220 offset:24576\n\t"
      "v_exp_f32 v211, v51\n\t"
      "v_mfma_f32_16x16x32_bf16 v[40:43], a[100:103], v[20:23], v[40:43]\n\t"
      "s_waitcnt lgkmcnt(4)\n\t"
      "v_add_f32 v216, v216, v209\n\t"
      "v_exp_f32 v212, v52\n\t"
      "v_cvt_pk_bf16_f32 v89, v210, v211\n\t"
      "v_add_f32 v216, v216, v210\n\t"
      "s_nop 6\n\t"
      "v_mfma_f32_16x16x32_bf16 v[32:35], a[104:107], v[8:11], v[32:35]\n\t"
      "ds_read_b128 a[68:71], v220 offset:25088\n\t"
      "v_exp_f32 v213, v53\n\t"
      "v_mfma_f32_16x16x32_bf16 v[40:43], a[104:107], v[24:27], v[40:43]\n\t"
      "v_add_f32 v216, v216, v211\n\t"
      "v_exp_f32 v214, v54\n\t"
      "v_cvt_pk_bf16_f32 v90, v212, v213\n\t"
      "v_add_f32 v216, v216, v212\n\t"
      "v_exp_f32 v215, v55\n\t"
      "s_nop 5\n\t"
      "v_mfma_f32_16x16x32_bf16 v[32:35], a[108:111], v[12:15], v[32:35]\n\t"
      "ds_read_b128 a[72:75], v220 offset:25600\n\t"
      "v_add_f32 v216, v216, v213\n\t"
      "v_mfma_f32_16x16x32_bf16 v[40:43], a[108:111], v[28:31], v[40:43]\n\t"
      "v_exp_f32 v217, v56\n\t"
      "v_cvt_pk_bf16_f32 v91, v214, v215\n\t"
      "v_add_f32 v216, v216, v214\n\t"
      "v_exp_f32 v209, v57\n\t"
      "v_add_f32 v216, v216, v215\n\t"
      "s_nop 6\n\t"
      "v_mfma_f32_16x16x32_bf16 v[36:39], a[112:115], v[0:3], v[64:67]\n\t"
      "ds_read_b128 a[76:79], v220 offset:26112\n\t"
      "v_exp_f32 v210, v58\n\t"
      "v_mfma_f32_16x16x32_bf16 v[44:47], a[112:115], v[16:19], v[68:71]\n\t"
      "s_add_u32 m0, %[sMW], 98304\n\t"
      "s_mov_b32 s40, 65536\n\t"
      "s_waitcnt lgkmcnt(4)\n\t"
      "v_cvt_pk_bf16_f32 v92, v217, v209\n\t"
      "v_exp_f32 v211, v59\n\t"
      "v_add_f32 v217, v217, v209\n\t"
      "s_nop 5\n\t"
      "v_mfma_f32_16x16x32_bf16 v[36:39], a[116:119], v[4:7], v[36:39]\n\t"
      "ds_read_b128 a[80:83], v220 offset:28672\n\t"
      "v_exp_f32 v212, v60\n\t"
      "v_mfma_f32_16x16x32_bf16 v[44:47], a[116:119], v[20:23], v[44:47]\n\t"
      "v_cvt_pk_bf16_f32 v93, v210, v211\n\t"
      "v_add_f32 v217, v217, v210\n\t"
      "v_exp_f32 v213, v61\n\t"
      "v_add_f32 v217, v217, v211\n\t"
      "v_exp_f32 v214, v62\n\t"
      "s_nop 5\n\t"
      "v_mfma_f32_16x16x32_bf16 v[36:39], a[120:123], v[8:11], v[36:39]\n\t"
      "ds_read_b128 a[84:87], v220 offset:29184\n\t"
      "v_cvt_pk_bf16_f32 v94, v212, v213\n\t"
      "v_mfma_f32_16x16x32_bf16 v[44:47], a[120:123], v[24:27], v[44:47]\n\t"
      "buffer_load_dwordx4 v224, %[sK], s40 offen offset:0 lds\n\t"
      "v_add_f32 v217, v217, v212\n\t"
      "v_exp_f32 v215, v63\n\t"
      "v_add_f32 v217, v217, v213\n\t"
      "v_add_f32 v217, v217, v214\n\t"
      "v_cvt_pk_bf16_f32 v95, v214, v215\n\t"
      "s_nop 6\n\t"
      "v_mfma_f32_16x16x32_bf16 v[36:39], a[124:127], v[12:15], v[36:39]\n\t"
      "ds_read_b128 a[88:91], v220 offset:29696\n\t"
      "v_add_f32 v217, v217, v215\n\t"
      "v_mfma_f32_16x16x32_bf16 v[44:47], a[124:127], v[28:31], v[44:47]\n\t"
      "buffer_load_dwordx4 v225, %[sK], s40 offen offset:1024 lds\n\t"
      "s_waitcnt lgkmcnt(4)\n\t"
      "v_add_f32 v72, v216, v217\n\t"
      "v_cmp_nge_f32 vcc, %[sLIM], v72\n\t"
      "s_cbranch_vccnz .Lr1_%=\n\t"
      ".Lb1_%=:\n\t"
      "v_add_f32 v218, v218, v216\n\t"
      "v_add_f32 v219, v219, v217\n\t"
      "s_nop 6\n\t"
      "v_mfma_f32_16x16x32_bf16 v[48:51], a[64:67], v[0:3], v[64:67]\n\t"
      "ds_read_b128 a[92:95], v220 offset:30208\n\t"
      "v_exp_f32 v216, v32\n\t"
      "v_mfma_f32_16x16x32_bf16 v[56:59], a[64:67], v[16:19], v[68:71]\n\t"
      "global_store_dwordx4 v233, v[200:203], %[sIN] offset:-1024 sc1\n\t"
      "v_exp_f32 v209, v33\n\t"
      "v_exp_f32 v210, v34\n\t"
      "v_cvt_pk_bf16_f32 v96, v216, v209\n\t"
      "v_exp_f32 v211, v35\n\t"
      "s_nop 4\n\t"
      "v_mfma_f32_16x16x32_bf16 v[48:51], a[68:71], v[4:7], v[48:51]\n\t"
      "v_mfma_f32_16x16x32_bf16 v[56:59], a[68:71], v[20:23], v[56:59]\n\t"
      "s_waitcnt vmcnt(13)\n\t"
      "s_barrier\n\t"
      "ds_read_b128 a[96:99], v220 offset:32768\n\t"
      "buffer_load_dwordx4 v226, %[sK], s40 offen offset:2048 lds\n\t"
      "v_add_f32 v216, v216, v209\n\t"
      "v_exp_f32 v212, v36\n\t"
      "v_cvt_pk_bf16_f32 v97, v210, v211\n\t"
      "s_nop 5\n\t"
      "v_mfma_f32_16x16x32_bf16 v[48:51], a[72:75], v[8:11], v[48:51]\n\t"
      "ds_read_b128 a[100:103], v220 offset:33280\n\t"
      "v_add_f32 v216, v216, v210\n\t"
      "v_mfma_f32_16x16x32_bf16 v[56:59], a[72:75], v[24:27], v[56:59]\n\t"
      "s_waitcnt lgkmcnt(4)\n\t"
      "v_exp_f32 v213, v37\n\t"
      "v_add_f32 v216, v216, v211\n\t"
      "v_exp_f32 v214, v38\n\t"
      "v_cvt_pk_bf16_f32 v98, v212, v213\n\t"
      "v_add_f32 v216, v216, v212\n\t"
      "s_nop 5\n\t"
      "v_mfma_f32_16x16x32_bf16 v[48:51], a[76:79], v[12:15], v[48:51]\n\t"
      "ds_read_b128 a[104:107], v220 offset:33792\n\t"
      "v_exp_f32 v215, v39\n\t"
      "v_mfma_f32_16x16x32_bf16 v[56:59], a[76:79], v[28:31], v[56:59]\n\t"
      "buffer_load_dwordx4 v227, %[sK], s40 offen offset:3072 lds\n\t"
      "v_add_f32 v216, v216, v213\n\t"
      "v_exp_f32 v217, v40\n\t"
      "v_cvt_pk_bf16_f32 v99, v214, v215\n\t"
      "v_add_f32 v216, v216, v214\n\t"
      "v_exp_f32 v209, v41\n\t"
      "s_nop 4\n\t"
      "v_mfma_f32_16x16x32_bf16 v[52:55], a[80:83], v[0:3], v[64:67]\n\t"
      "ds_read_b128 a[108:111], v220 offset:34304\n\t"
      "v_add_f32 v216, v216, v215\n\t"
      "v_mfma_f32_16x16x32_bf16 v[60:63], a[80:83], v[16:19], v[68:71]\n\t"
      "v_exp_f32 v210, v42\n\t"
      "v_cvt_pk_bf16_f32 v100, v217, v209\n\t"
      "v_exp_f32 v211, v43\n\t"
      "v_add_f32 v217, v217, v209\n\t"
      "v_exp_f32 v212, v44\n\t"
      "s_nop 5\n\t"
      "v_mfma_f32_16x16x32_bf16 v[52:55], a[84:87], v[4:7], v[52:55]\n\t"
      "v_mfma_f32_16x16x32_bf16 v[60:63], a[84:87], v[20:23], v[60:63]\n\t"
      "s_waitcnt vmcnt(11) lgkmcnt(3)\n\t"
      "s_barrier\n\t"
      "ds_read_b64_tr_b16 a[112:113], v221 offset:512\n\t"
      "ds_read_b64_tr_b16 a[114:115], v221 offset:2560\n\t"
      "v_cvt_pk_bf16_f32 v101, v210, v211\n\t"
      "v_add_f32 v217, v217, v210\n\t"
      "v_exp_f32 v213, v45\n\t"
      "s_nop 5\n\t"
      "v_mfma_f32_16x16x32_bf16 v[52:55], a[88:91], v[8:11], v[52:55]\n\t"
      "ds_read_b128 a[116:119], v220 offset:36864\n\t"
      "v_add_f32 v217, v217, v211\n\t"
      "v_mfma_f32_16x16x32_bf16 v[60:63], a[88:91], v[24:27], v[60:63]\n\t"
      "s_add_u32 m0, %[sMW], 16384\n\t"
      "s_mov_b32 s40, 32768\n\t"
      "v_exp_f32 v214, v46\n\t"
      "v_cvt_pk_bf16_f32 v102, v212, v213\n\t"
      "v_add_f32 v217, v217, v212\n\t"
      "v_exp_f32 v215, v47\n\t"
      "s_nop 5\n\t"
      "v_mfma_f32_16x16x32_bf16 v[52:55], a[92:95], v[12:15], v[52:55]\n\t"
      "ds_read_b128 a[120:123], v220 offset:37376\n\t"
      "v_add_f32 v217, v217, v213\n\t"
      "v_mfma_f32_16x16x32_bf16 v[60:63], a[92:95], v[28:31], v[60:63]\n\t"
      "v_add_f32 v217, v217, v214\n\t"
      "v_cvt_pk_bf16_f32 v103, v214, v215\n\t"
      "v_add_f32 v217, v217, v215\n\t"
      "v_add_f32 v72, v216, v217\n\t"
      "v_cmp_nge_f32 vcc, %[sLIM], v72\n\t"
      "s_cbranch_vccnz .Lr2_%=\n\t"
      ".Lb2_%=:\n\t"
      "v_add_f32 v218, v218, v216\n\t"
      "v_add_f32 v219, v219, v217\n\t"
      "s_nop 5\n\t"
      "v_mfma_f32_16x16x32_bf16 v[32:35], a[96:99], v[0:3], v[64:67]\n\t"
      "ds_read_b128 a[124:127], v220 offset:37888\n\t"
      "v_exp_f32 v216, v48\n\t"
      "v_mfma_f32_16x16x32_bf16 v[40:43], a[96:99], v[16:19], v[68:71]\n\t"
      "buffer_load_dwordx4 v239, %[sVH], s40 offen offset:0 lds\n\t"
      "global_store_dwordx4 v233, v[200:203], %[sIN] offset:0 sc1\n\t"
      "s_waitcnt lgkmcnt(5)\n\t"
      "v_exp_f32 v209, v49\n\t"
      "s_nop 3\n\t"
      "v_mfma_f32_16x16x32_bf16 v[32:35], a[100:103], v[4:7], v[32:35]\n\t"
      "ds_read_b128 a[64:67], v220 offset:38400\n\t"
      "v_exp_f32 v210, v50\n\t"
      "v_mfma_f32_16x16x32_bf16 v[40:43], a[100:103], v[20:23], v[40:43]\n\t"
      "buffer_load_dwordx4 v235, %[sVH], s40 offen offset:1024 lds\n\t"
      "v_cvt_pk_bf16_f32 v104, v216, v209\n\t"
      "v_exp_f32 v211, v51\n\t"
      "v_add_f32 v216, v216, v209\n\t"
      "s_nop 3\n\t"
      "v_mfma_f32_16x16x32_bf16 v[32:35], a[104:107], v[8:11], v[32:35]\n\t"
      "ds_read_b64_tr_b16 a[68:69], v222 offset:512\n\t"
      "v_exp_f32 v212, v52\n\t"
      "v_mfma_f32_16x16x32_bf16 v[40:43], a[104:107], v[24:27], v[40:43]\n\t"
      "ds_read_b64_tr_b16 a[70:71], v222 offset:2560\n\t"
      "v_cvt_pk_bf16_f32 v105, v210, v211\n\t"
      "v_add_f32 v216, v216, v210\n\t"
      "v_exp_f32 v213, v53\n\t"
      "v_add_f32 v216, v216, v211\n\t"
      "s_nop 2\n\t"
      "v_mfma_f32_16x16x32_bf16 v[32:35], a[108:111], v[12:15], v[32:35]\n\t"
      "ds_read_b128 a[72:75], v220 offset:40960\n\t"
      "v_exp_f32 v214, v54\n\t"
      "v_mfma_f32_16x16x32_bf16 v[40:43], a[108:111], v[28:31], v[40:43]\n\t"
      "buffer_load_dwordx4 v246, %[sVH], s40 offen offset:2048 lds\n\t"
      "s_waitcnt lgkmcnt(5)\n\t"
      "v_cvt_pk_bf16_f32 v106, v212, v213\n\t"
      "v_add_f32 v216, v216, v212\n\t"
      "s_nop 4\n\t"
      "v_mfma_f32_16x16x32_bf16 a[48:51], a[112:115], v[80:83], 0\n\t"
      "ds_read_b128 a[76:79], v220 offset:41472\n\t"
      "v_exp_f32 v215, v55\n\t"
      "v_mfma_f32_16x16x32_bf16 a[56:59], a[112:115], v[84:87], 0\n\t"
      "v_add_f32 v216, v216, v213\n\t"
      "v_exp_f32 v217, v56\n\t"
      "v_cvt_pk_bf16_f32 v107, v214, v215\n\t"
      "v_add_f32 v216, v216, v214\n\t"
      "s_nop 3\n\t"
      "v_mfma_f32_16x16x32_bf16 v[36:39], a[116:119], v[0:3], v[64:67]\n\t"
      "ds_read_b128 a[80:83], v220 offset:41984\n\t"
      "v_exp_f32 v209, v57\n\t"
      "v_mfma_f32_16x16x32_bf16 v[44:47], a[116:119], v[16:19], v[68:71]\n\t"
      "buffer_load_dwordx4 v247, %[sVH], s40 offen offset:3072 lds\n\t"
      "v_add_f32 v216, v216, v215\n\t"
      "v_exp_f32 v210, v58\n\t"
      "v_cvt_pk_bf16_f32 v108, v217, v209\n\t"
      "s_nop 3\n\t"
      "v_mfma_f32_16x16x32_bf16 v[36:39], a[120:123], v[4:7], v[36:39]\n\t"
      "ds_read_b128 a[84:87], v220 offset:42496\n\t"
      "v_exp_f32 v211, v59\n\t"
      "v_mfma_f32_16x16x32_bf16 v[44:47], a[120:123], v[20:23], v[44:47]\n\t"
      "s_waitcnt lgkmcnt(4)\n\t"
      "v_add_f32 v217, v217, v209\n\t"
      "v_exp_f32 v212, v60\n\t"
      "v_cvt_pk_bf16_f32 v109, v210, v211\n\t"
      "s_nop 3\n\t"
      "v_mfma_f32_16x16x32_bf16 v[36:39], a[124:127], v[8:11], v[36:39]\n\t"
      "ds_read_b64_tr_b16 a[88:89], v221 offset:4608\n\t"
      "v_add_f32 v217, v217, v210\n\t"
      "v_mfma_f32_16x16x32_bf16 v[44:47], a[124:127], v[24:27], v[44:47]\n\t"
      "ds_read_b64_tr_b16 a[90:91], v221 offset:6656\n\t"
      "v_exp_f32 v213, v61\n\t"
      "v_add_f32 v217, v217, v211\n\t"
      "v_exp_f32 v214, v62\n\t"
      "s_nop 3\n\t"
      "v_mfma_f32_16x16x32_bf16 v[36:39], a[64:67], v[12:15], v[36:39]\n\t"
      "ds_read_b128 a[92:95], v220 offset:45056\n\t"
      "v_cvt_pk_bf16_f32 v110, v212, v213\n\t"
      "v_mfma_f32_16x16x32_bf16 v[44:47], a[64:67], v[28:31], v[44:47]\n\t"
      "v_add_f32 v217, v217, v212\n\t"
      "v_exp_f32 v215, v63\n\t"
      "v_add_f32 v217, v217, v213\n\t"
      "v_add_f32 v217, v217, v214\n\t"
      "v_cvt_pk_bf16_f32 v111, v214, v215\n\t"
      "s_nop 3\n\t"
      "v_mfma_f32_16x16x32_bf16 a[52:55], a[68:71], v[80:83], 0\n\t"
      "ds_read_b128 a[96:99], v220 offset:45568\n\t"
      "v_add_f32 v217, v217, v215\n\t"
      "v_mfma_f32_16x16x32_bf16 a[60:63], a[68:71], v[84:87], 0\n\t"
      "s_waitcnt lgkmcnt(5)\n\t"
      "v_add_f32 v72, v216, v217\n\t"
      "v_cmp_nge_f32 vcc, %[sLIM], v72\n\t"
      "s_cbranch_vccnz .Lr3_%=\n\t"
      ".Lb3_%=:\n\t"
      "v_add_f32 v218, v218, v216\n\t"
      "v_add_f32 v219, v219, v217\n\t"
      "s_nop 3\n\t"
      "v_mfma_f32_16x16x32_bf16 v[48:51], a[72:75], v[0:3], v[64:67]\n\t"
      "ds_read_b128 a[100:103], v220 offset:46080\n\t"
      "v_exp_f32 v216, v32\n\t"
      "v_mfma_f32_16x16x32_bf16 v[56:59], a[72:75], v[16:19], v[68:71]\n\t"
      "global_store_dwordx4 v233, v[200:203], %[sIN] offset:1024 sc1\n\t"
      "v_exp_f32 v209, v33\n\t"
      "v_exp_f32 v210, v34\n\t"
      "v_cvt_pk_bf16_f32 v112, v216, v209\n\t"
      "s_nop 2\n\t"
      "v_mfma_f32_16x16x32_bf16 v[48:51], a[76:79], v[4:7], v[48:51]\n\t"
      "ds_read_b128 a[104:107], v220 offset:46592\n\t"
      "v_exp_f32 v211, v35\n\t"
      "v_mfma_f32_16x16x32_bf16 v[56:59], a[76:79], v[20:23], v[56:59]\n\t"
      "s_add_u32 m0, %[sMW], 114688\n\t"
      "s_mov_b32 s40, 81920\n\t"
      "v_add_f32 v216, v216, v209\n\t"
      "v_exp_f32 v212, v36\n\t"
      "v_cvt_pk_bf16_f32 v113, v210, v211\n\t"
      "s_nop 2\n\t"
      "v_mfma_f32_16x16x32_bf16 v[48:51], a[80:83], v[8:11], v[48:51]\n\t"
      "ds_read_b64_tr_b16 a[108:109], v222 offset:4608\n\t"
      "v_add_f32 v216, v216, v210\n\t"
      "v_mfma_f32_16x16x32_bf16 v[56:59], a[80:83], v[24:27], v[56:59]\n\t"
      "ds_read_b64_tr_b16 a[110:111], v222 offset:6656\n\t"
      "s_waitcnt lgkmcnt(5)\n\t"
      "v_exp_f32 v213, v37\n\t"
      "v_add_f32 v216, v216, v211\n\t"
      "v_exp_f32 v214, v38\n\t"
      "s_nop 2\n\t"
      "v_mfma_f32_16x16x32_bf16 v[48:51], a[84:87], v[12:15], v[48:51]\n\t"
      "v_mfma_f32_16x16x32_bf16 v[56:59], a[84:87], v[28:31], v[56:59]\n\t"
      "s_waitcnt vmcnt(12)\n\t"
      "s_barrier\n\t"
      "ds_read_b128 a[112:115], v220 offset:49152\n\t"
      "buffer_load_dwordx4 v224, %[sK], s40 offen offset:0 lds\n\t"
      "v_cvt_pk_bf16_f32 v114, v212, v213\n\t"
      "v_add_f32 v216, v216, v212\n\t"
      "s_nop 3\n\t"
      "v_mfma_f32_16x16x32_bf16 a[48:51], a[88:91], v[88:91], a[48:51]\n\t"
      "ds_read_b128 a[116:119], v220 offset:49664\n\t"
      "v_exp_f32 v215, v39\n\t"
      "v_mfma_f32_16x16x32_bf16 a[56:59], a[88:91], v[92:95], a[56:59]\n\t"
      "buffer_load_dwordx4 v225, %[sK], s40 offen offset:1024 lds\n\t"
      "v_add_f32 v216, v216, v213\n\t"
      "v_exp_f32 v217, v40\n\t"
      "v_cvt_pk_bf16_f32 v115, v214, v215\n\t"
      "v_add_f32 v216, v216, v214\n\t"
      "s_nop 2\n\t"
      "v_mfma_f32_16x16x32_bf16 v[52:55], a[92:95], v[0:3], v[64:67]\n\t"
      "ds_read_b64_tr_b16 a[120:121], v221 offset:8704\n\t"
      "v_exp_f32 v209, v41\n\t"
      "v_mfma_f32_16x16x32_bf16 v[60:63], a[92:95], v[16:19], v[68:71]\n\t"
      "ds_read_b64_tr_b16 a[122:123], v221 offset:10752\n\t"
      "s_waitcnt lgkmcnt(6)\n\t"
      "v_add_f32 v216, v216, v215\n\t"
      "v_exp_f32 v210, v42\n\t"
      "s_nop 3\n\t"
      "v_mfma_f32_16x16x32_bf16 v[52:55], a[96:99], v[4:7], v[52:55]\n\t"
      "ds_read_b128 a[124:127], v220 offset:50176\n\t"
      "v_cvt_pk_bf16_f32 v116, v217, v209\n\t"
      "v_mfma_f32_16x16x32_bf16 v[60:63], a[96:99], v[20:23], v[60:63]\n\t"
      "buffer_load_dwordx4 v226, %[sK], s40 offen offset:2048 lds\n\t"
      "v_exp_f32 v211, v43\n\t"
      "v_add_f32 v217, v217, v209\n\t"
      "v_exp_f32 v212, v44\n\t"
      "v_cvt_pk_bf16_f32 v117, v210, v211\n\t"
      "s_nop 2\n\t"
      "v_mfma_f32_16x16x32_bf16 v[52:55], a[100:103], v[8:11], v[52:55]\n\t"
      "ds_read_b128 a[64:67], v220 offset:50688\n\t"
      "v_add_f32 v217, v217, v210\n\t"
      "v_mfma_f32_16x16x32_bf16 v[60:63], a[100:103], v[24:27], v[60:63]\n\t"
      "v_exp_f32 v213, v45\n\t"
      "v_add_f32 v217, v217, v211\n\t"
      "v_exp_f32 v214, v46\n\t"
      "v_cvt_pk_bf16_f32 v118, v212, v213\n\t"
      "s_nop 3\n\t"
      "v_mfma_f32_16x16x32_bf16 v[52:55], a[104:107], v[12:15], v[52:55]\n\t"
      "ds_read_b128 a[68:71], v220 offset:53248\n\t"
      "v_add_f32 v217, v217, v212\n\t"
      "v_mfma_f32_16x16x32_bf16 v[60:63], a[104:107], v[28:31], v[60:63]\n\t"
      "buffer_load_dwordx4 v227, %[sK], s40 offen offset:3072 lds\n\t"
      "s_waitcnt lgkmcnt(5)\n\t"
      "v_exp_f32 v215, v47\n\t"
      "v_add_f32 v217, v217, v213\n\t"
      "v_add_f32 v217, v217, v214\n\t"
      "v_cvt_pk_bf16_f32 v119, v214, v215\n\t"
      "s_nop 2\n\t"
      "v_mfma_f32_16x16x32_bf16 a[52:55], a[108:111], v[88:91], a[52:55]\n\t"
      "ds_read_b64_tr_b16 a[72:73], v222 offset:8704\n\t"
      "v_add_f32 v217, v217, v215\n\t"
      "v_mfma_f32_16x16x32_bf16 a[60:63], a[108:111], v[92:95], a[60:63]\n\t"
      "ds_read_b64_tr_b16 a[74:75], v222 offset:10752\n\t"
      "v_add_f32 v72, v216, v217\n\t"
      "v_cmp_nge_f32 vcc, %[sLIM], v72\n\t"
      "s_cbranch_vccnz .Lr4_%=\n\t"
      ".Lb4_%=:\n\t"
      "v_add_f32 v218, v218, v216\n\t"
      "v_add_f32 v219, v219, v217\n\t"
      "s_nop 3\n\t"
      "v_mfma_f32_16x16x32_bf16 v[32:35], a[112:115], v[0:3], v[64:67]\n\t"
      "ds_read_b128 a[76:79], v220 offset:53760\n\t"
      "v_exp_f32 v216, v48\n\t"
      "v_mfma_f32_16x16x32_bf16 v[40:43], a[112:115], v[16:19], v[68:71]\n\t"
      "global_store_dwordx4 v233, v[200:203], %[sIN] offset:2048 sc1\n\t"
      "v_exp_f32 v209, v49\n\t"
      "v_exp_f32 v210, v50\n\t"
      "s_nop 2\n\t"
      "v_mfma_f32_16x16x32_bf16 v[32:35], a[116:119], v[4:7], v[32:35]\n\t"
      "ds_read_b128 a[80:83], v220 offset:54272\n\t"
      "v_cvt_pk_bf16_f32 v120, v216, v209\n\t"
      "v_mfma_f32_16x16x32_bf16 v[40:43], a[116:119], v[20:23], v[40:43]\n\t"
      "s_waitcnt lgkmcnt(5)\n\t"
      "v_exp_f32 v211, v51\n\t"
      "v_add_f32 v216, v216, v209\n\t"
      "v_exp_f32 v212, v52\n\t"
      "s_nop 2\n\t"
      "v_mfma_f32_16x16x32_bf16 a[48:51], a[120:123], v[96:99], a[48:51]\n\t"
      "ds_read_b128 a[84:87], v220 offset:54784\n\t"
      "v_cvt_pk_bf16_f32 v121, v210, v211\n\t"
      "v_mfma_f32_16x16x32_bf16 a[56:59], a[120:123], v[100:103], a[56:59]\n\t"
      "v_add_f32 v216, v216, v210\n\t"
      "v_exp_f32 v213, v53\n\t"
      "v_add_f32 v216, v216, v211\n\t"
      "v_exp_f32 v214, v54\n\t"
      "s_nop 2\n\t"
      "v_mfma_f32_16x16x32_bf16 v[32:35], a[124:127], v[8:11], v[32:35]\n\t"
      "ds_read_b64_tr_b16 a[88:89], v221 offset:12800\n\t"
      "v_cvt_pk_bf16_f32 v122, v212, v213\n\t"
      "v_mfma_f32_16x16x32_bf16 v[40:43], a[124:127], v[24:27], v[40:43]\n\t"
      "ds_read_b64_tr_b16 a[90:91], v221 offset:14848\n\t"
      "v_add_f32 v216, v216, v212\n\t"
      "v_exp_f32 v215, v55\n\t"
      "v_add_f32 v216, v216, v213\n\t"
      "s_nop 3\n\t"
      "v_mfma_f32_16x16x32_bf16 v[32:35], a[64:67], v[12:15], v[32:35]\n\t"
      "ds_read_b128 a[92:95], v220 offset:57344\n\t"
      "v_exp_f32 v217, v56\n\t"
      "v_mfma_f32_16x16x32_bf16 v[40:43], a[64:67], v[28:31], v[40:43]\n\t"
      "s_add_u32 m0, %[sMW], 131072\n\t"
      "s_mov_b32 s40, 98304\n\t"
      "s_waitcnt lgkmcnt(5)\n\t"
      "v_cvt_pk_bf16_f32 v123, v214, v215\n\t"
      "s_nop 3\n\t"
      "v_mfma_f32_16x16x32_bf16 v[36:39], a[68:71], v[0:3], v[64:67]\n\t"
      "ds_read_b128 a[96:99], v220 offset:57856\n\t"
      "v_add_f32 v216, v216, v214\n\t"
      "v_mfma_f32_16x16x32_bf16 v[44:47], a[68:71], v[16:19], v[68:71]\n\t"
      "v_exp_f32 v209, v57\n\t"
      "v_add_f32 v216, v216, v215\n\t"
      "v_exp_f32 v210, v58\n\t"
      "v_cvt_pk_bf16_f32 v124, v217, v209\n\t"
      "s_nop 2\n\t"
      "v_mfma_f32_16x16x32_bf16 a[52:55], a[72:75], v[96:99], a[52:55]\n\t"
      "ds_read_b64_tr_b16 a[100:101], v222 offset:12800\n\t"
      "v_exp_f32 v211, v59\n\t"
      "v_mfma_f32_16x16x32_bf16 a[60:63], a[72:75], v[100:103], a[60:63]\n\t"
      "ds_read_b64_tr_b16 a[102:103], v222 offset:14848\n\t"
      "buffer_load_dwordx4 v224, %[sK], s40 offen offset:0 lds\n\t"
      "v_add_f32 v217, v217, v209\n\t"
      "v_exp_f32 v212, v60\n\t"
      "s_nop 2\n\t"
      "v_mfma_f32_16x16x32_bf16 v[36:39], a[76:79], v[4:7], v[36:39]\n\t"
      "ds_read_b128 a[104:107], v220 offset:58368\n\t"
      "v_cvt_pk_bf16_f32 v125, v210, v211\n\t"
      "v_mfma_f32_16x16x32_bf16 v[44:47], a[76:79], v[20:23], v[44:47]\n\t"
      "buffer_load_dwordx4 v225, %[sK], s40 offen offset:1024 lds\n\t"
      "s_waitcnt lgkmcnt(5)\n\t"
      "v_add_f32 v217, v217, v210\n\t"
      "v_exp_f32 v213, v61\n\t"
      "s_nop 3\n\t"
      "v_mfma_f32_16x16x32_bf16 v[36:39], a[80:83], v[8:11], v[36:39]\n\t"
      "ds_read_b128 a[108:111], v220 offset:58880\n\t"
      "v_add_f32 v217, v217, v211\n\t"
      "v_mfma_f32_16x16x32_bf16 v[44:47], a[80:83], v[24:27], v[44:47]\n\t"
      "v_exp_f32 v214, v62\n\t"
      "v_cvt_pk_bf16_f32 v126, v212, v213\n\t"
      "v_add_f32 v217, v217, v212\n\t"
      "v_exp_f32 v215, v63\n\t"
      "s_nop 2\n\t"
      "v_mfma_f32_16x16x32_bf16 v[36:39], a[84:87], v[12:15], v[36:39]\n\t"
      "ds_read_b128 a[112:115], v220 offset:61440\n\t"
      "v_add_f32 v217, v217, v213\n\t"
      "v_mfma_f32_16x16x32_bf16 v[44:47], a[84:87], v[28:31], v[44:47]\n\t"
      "buffer_load_dwordx4 v226, %[sK], s40 offen offset:2048 lds\n\t"
      "v_add_f32 v217, v217, v214\n\t"
      "v_cvt_pk_bf16_f32 v127, v214, v215\n\t"
      "v_add_f32 v217, v217, v215\n\t"
      "v_add_f32 v72, v216, v217\n\t"
      "s_nop 3\n\t"
      "v_mfma_f32_16x16x32_bf16 a[48:51], a[88:91], v[104:107], a[48:51]\n\t"
      "ds_read_b64_tr_b16 a[116:117], v221 offset:0\n\t"
      "s_waitcnt lgkmcnt(4)\n\t"
      "v_mfma_f32_16x16x32_bf16 a[56:59], a[88:91], v[108:111], a[56:59]\n\t"
      "ds_read_b64_tr_b16 a[118:119], v221 offset:2048\n\t"
      "v_cmp_nge_f32 vcc, %[sLIM], v72\n\t"
      "s_cbranch_vccnz .Lr5_%=\n\t"
      ".Lb5_%=:\n\t"
      "v_add_f32 v218, v218, v216\n\t"
      "v_add_f32 v219, v219, v217\n\t"
      "s_nop 3\n\t"
      "v_mfma_f32_16x16x32_bf16 v[48:51], a[92:95], v[0:3], v[64:67]\n\t"
      "ds_read_b128 a[120:123], v220 offset:61952\n\t"
      "v_exp_f32 v216, v32\n\t"
      "v_mfma_f32_16x16x32_bf16 v[56:59], a[92:95], v[16:19], v[68:71]\n\t"
      "buffer_load_dwordx4 v227, %[sK], s40 offen offset:3072 lds\n\t"
      "global_store_dwordx4 v233, v[200:203], %[sIN] offset:3072 sc1\n\t"
      "v_exp_f32 v209, v33\n\t"
      "v_exp_f32 v210, v34\n\t"
      "s_nop 1\n\t"
      "v_mfma_f32_16x16x32_bf16 v[48:51], a[96:99], v[4:7], v[48:51]\n\t"
      "ds_read_b128 a[124:127], v220 offset:62464\n\t"
      "v_cvt_pk_bf16_f32 v128, v216, v209\n\t"
      "v_mfma_f32_16x16x32_bf16 v[56:59], a[96:99], v[20:23], v[56:59]\n\t"
      "v_exp_f32 v211, v35\n\t"
      "v_add_f32 v216, v216, v209\n\t"
      "v_exp_f32 v212, v36\n\t"
      "v_cvt_pk_bf16_f32 v129, v210, v211\n\t"
      "s_nop 2\n\t"
      "v_mfma_f32_16x16x32_bf16 a[52:55], a[100:103], v[104:107], a[52:55]\n\t"
      "ds_read_b128 a[64:67], v220 offset:62976\n\t"
      "v_add_f32 v216, v216, v210\n\t"
      "v_mfma_f32_16x16x32_bf16 a[60:63], a[100:103], v[108:111], a[60:63]\n\t"
      "s_waitcnt lgkmcnt(5)\n\t"
      "v_exp_f32 v213, v37\n\t"
      "v_add_f32 v216, v216, v211\n\t"
      "v_exp_f32 v214, v38\n\t"
      "s_nop 2\n\t"
      "v_mfma_f32_16x16x32_bf16 v[48:51], a[104:107], v[8:11], v[48:51]\n\t"
      "ds_read_b64_tr_b16 a[68:69], v222 offset:0\n\t"
      "v_cvt_pk_bf16_f32 v130, v212, v213\n\t"
      "v_mfma_f32_16x16x32_bf16 v[56:59], a[104:107], v[24:27], v[56:59]\n\t"
      "ds_read_b64_tr_b16 a[70:71], v222 offset:2048\n\t"
      "v_add_f32 v216, v216, v212\n\t"
      "v_exp_f32 v215, v39\n\t"
      "v_add_f32 v216, v216, v213\n\t"
      "v_exp_f32 v217, v40\n\t"
      "v_add_u32 v220, 0x10000, v220\n\t"
      "s_nop 1\n\t"
      "v_mfma_f32_16x16x32_bf16 v[48:51], a[108:111], v[12:15], v[48:51]\n\t"
      "v_mfma_f32_16x16x32_bf16 v[56:59], a[108:111], v[28:31], v[56:59]\n\t"
      "s_waitcnt vmcnt(16)\n\t"
      "s_barrier\n\t"
      "ds_read_b128 a[72:75], v220 offset:0\n\t"
      "v_cvt_pk_bf16_f32 v131, v214, v215\n\t"
      "v_add_f32 v216, v216, v214\n\t"
      "s_nop 3\n\t"
      "v_mfma_f32_16x16x32_bf16 v[52:55], a[112:115], v[0:3], v[64:67]\n\t"
      "ds_read_b128 a[76:79], v220 offset:512\n\t"
      "v_exp_f32 v209, v41\n\t"
      "v_mfma_f32_16x16x32_bf16 v[60:63], a[112:115], v[16:19], v[68:71]\n\t"
      "s_waitcnt lgkmcnt(5)\n\t"
      "v_add_f32 v216, v216, v215\n\t"
      "v_exp_f32 v210, v42\n\t"
      "v_cvt_pk_bf16_f32 v132, v217, v209\n\t"
      "s_nop 2\n\t"
      "v_mfma_f32_16x16x32_bf16 a[32:35], a[116:119], v[80:83], 0\n\t"
      "v_mfma_f32_16x16x32_bf16 a[40:43], a[116:119], v[84:87], 0\n\t"
      "s_waitcnt vmcnt(11)\n\t"
      "s_barrier\n\t"
      "ds_read_b64_tr_b16 a[80:81], v221 offset:16896\n\t"
      "ds_read_b64_tr_b16 a[82:83], v221 offset:18944\n\t"
      "s_add_u32 m0, %[sMW], 32768\n\t"
      "s_mov_b32 s40, 65536\n\t"
      "s_nop 2\n\t"
      "v_mfma_f32_16x16x32_bf16 v[52:55], a[120:123], v[4:7], v[52:55]\n\t"
      "ds_read_b128 a[84:87], v220 offset:1024\n\t"
      "v_exp_f32 v211, v43\n\t"
      "v_mfma_f32_16x16x32_bf16 v[60:63], a[120:123], v[20:23], v[60:63]\n\t"
      "v_add_f32 v217, v217, v209\n\t"
      "v_exp_f32 v212, v44\n\t"
      "v_cvt_pk_bf16_f32 v133, v210, v211\n\t"
      "v_add_f32 v217, v217, v210\n\t"
      "s_nop 2\n\t"
      "v_mfma_f32_16x16x32_bf16 v[52:55], a[124:127], v[8:11], v[52:55]\n\t"
      "ds_read_b128 a[88:91], v220 offset:1536\n\t"
      "v_exp_f32 v213, v45\n\t"
      "v_mfma_f32_16x16x32_bf16 v[60:63], a[124:127], v[24:27], v[60:63]\n\t"
      "buffer_load_dwordx4 v239, %[sVH], s40 offen offset:0 lds\n\t"
      "s_waitcnt lgkmcnt(5)\n\t"
      "v_add_f32 v217, v217, v211\n\t"
      "v_exp_f32 v214, v46\n\t"
      "v_cvt_pk_bf16_f32 v134, v212, v213\n\t"
      "s_nop 1\n\t"
      "v_mfma_f32_16x16x32_bf16 v[52:55], a[64:67], v[12:15], v[52:55]\n\t"
      "ds_read_b128 a[92:95], v220 offset:4096\n\t"
      "v_add_f32 v217, v217, v212\n\t"
      "v_mfma_f32_16x16x32_bf16 v[60:63], a[64:67], v[28:31], v[60:63]\n\t"
      "buffer_load_dwordx4 v235, %[sVH], s40 offen offset:1024 lds\n\t"
      "v_exp_f32 v215, v47\n\t"
      "v_add_f32 v217, v217, v213\n\t"
      "v_add_f32 v217, v217, v214\n\t"
      "v_cvt_pk_bf16_f32 v135, v214, v215\n\t"
      "s_nop 2\n\t"
      "v_mfma_f32_16x16x32_bf16 a[36:39], a[68:71], v[80:83], 0\n\t"
      "ds_read_b64_tr_b16 a[96:97], v222 offset:16896\n\t"
      "v_add_f32 v217, v217, v215\n\t"
      "v_mfma_f32_16x16x32_bf16 a[44:47], a[68:71], v[84:87], 0\n\t"
      "ds_read_b64_tr_b16 a[98:99], v222 offset:18944\n\t"
      "v_add_f32 v72, v216, v217\n\t"
      "v_cmp_nge_f32 vcc, %[sLIM], v72\n\t"
      "s_cbranch_vccnz .Lr6_%=\n\t"
      ".Lb6_%=:\n\t"
      "v_add_f32 v218, v218, v216\n\t"
      "v_add_f32 v219, v219, v217\n\t"
      "s_nop 2\n\t"
      "v_mfma_f32_16x16x32_bf16 v[32:35], a[72:75], v[0:3], v[64:67]\n\t"
      "ds_read_b128 a[100:103], v220 offset:4608\n\t"
      "v_exp_f32 v216, v48\n\t"
      "v_mfma_f32_16x16x32_bf16 v[40:43], a[72:75], v[16:19], v[68:71]\n\t"
      "buffer_load_dwordx4 v246, %[sVH], s40 offen offset:2048 lds\n\t"
      "s_waitcnt lgkmcnt(5)\n\t"
      "v_exp_f32 v209, v49\n\t"
      "s_nop 3\n\t"
      "v_mfma_f32_16x16x32_bf16 v[32:35], a[76:79], v[4:7], v[32:35]\n\t"
      "ds_read_b128 a[104:107], v220 offset:5120\n\t"
      "v_exp_f32 v210, v50\n\t"
      "v_mfma_f32_16x16x32_bf16 v[40:43], a[76:79], v[20:23], v[40:43]\n\t"
      "v_cvt_pk_bf16_f32 v136, v216, v209\n\t"
      "v_exp_f32 v211, v51\n\t"
      "v_add_f32 v216, v216, v209\n\t"
      "v_exp_f32 v212, v52\n\t"
      "s_nop 1\n\t"
      "v_mfma_f32_16x16x32_bf16 a[48:51], a[80:83], v[112:115], a[48:51]\n\t"
      "ds_read_b128 a[108:111], v220 offset:5632\n\t"
      "v_cvt_pk_bf16_f32 v137, v210, v211\n\t"
      "v_mfma_f32_16x16x32_bf16 a[56:59], a[80:83], v[116:119], a[56:59]\n\t"
      "buffer_load_dwordx4 v247, %[sVH], s40 offen offset:3072 lds\n\t"
      "v_add_f32 v216, v216, v210\n\t"
      "v_exp_f32 v213, v53\n\t"
      "v_add_f32 v216, v216, v211\n\t"
      "s_nop 3\n\t"
      "v_mfma_f32_16x16x32_bf16 v[32:35], a[84:87], v[8:11], v[32:35]\n\t"
      "ds_read_b64_tr_b16 a[112:113], v221 offset:20992\n\t"
      "v_exp_f32 v214, v54\n\t"
      "v_mfma_f32_16x16x32_bf16 v[40:43], a[84:87], v[24:27], v[40:43]\n\t"
      "ds_read_b64_tr_b16 a[114:115], v221 offset:23040\n\t"
      "s_waitcnt lgkmcnt(5)\n\t"
      "v_cvt_pk_bf16_f32 v138, v212, v213\n\t"
      "v_add_f32 v216, v216, v212\n\t"
      "s_nop 3\n\t"
      "v_mfma_f32_16x16x32_bf16 v[32:35], a[88:91], v[12:15], v[32:35]\n\t"
      "ds_read_b128 a[116:119], v220 offset:8192\n\t"
      "v_exp_f32 v215, v55\n\t"
      "v_mfma_f32_16x16x32_bf16 v[40:43], a[88:91], v[28:31], v[40:43]\n\t"
      "v_add_f32 v216, v216, v213\n\t"
      "v_exp_f32 v217, v56\n\t"
      "v_cvt_pk_bf16_f32 v139, v214, v215\n\t"
      "v_add_f32 v216, v216, v214\n\t"
      "s_nop 2\n\t"
      "v_mfma_f32_16x16x32_bf16 v[36:39], a[92:95], v[0:3], v[64:67]\n\t"
      "ds_read_b128 a[120:123], v220 offset:8704\n\t"
      "v_exp_f32 v209, v57\n\t"
      "v_mfma_f32_16x16x32_bf16 v[44:47], a[92:95], v[16:19], v[68:71]\n\t"
      "v_add_f32 v216, v216, v215\n\t"
      "v_exp_f32 v210, v58\n\t"
      "v_cvt_pk_bf16_f32 v140, v217, v209\n\t"
      "s_nop 3\n\t"
      "v_mfma_f32_16x16x32_bf16 a[52:55], a[96:99], v[112:115], a[52:55]\n\t"
      "ds_read_b64_tr_b16 a[124:125], v222 offset:20992\n\t"
      "v_exp_f32 v211, v59\n\t"
      "v_mfma_f32_16x16x32_bf16 a[60:63], a[96:99], v[116:119], a[60:63]\n\t"
      "ds_read_b64_tr_b16 a[126:127], v222 offset:23040\n\t"
      "s_add_u32 m0, %[sMW], 147456\n\t"
      "s_mov_b32 s40, 114688\n\t"
      "s_waitcnt lgkmcnt(6)\n\t"
      "v_add_f32 v217, v217, v209\n\t"
      "s_nop 2\n\t"
      "v_mfma_f32_16x16x32_bf16 v[36:39], a[100:103], v[4:7], v[36:39]\n\t"
      "ds_read_b128 a[64:67], v220 offset:9216\n\t"
      "v_exp_f32 v212, v60\n\t"
      "v_mfma_f32_16x16x32_bf16 v[44:47], a[100:103], v[20:23], v[44:47]\n\t"
      "v_cvt_pk_bf16_f32 v141, v210, v211\n\t"
      "v_add_f32 v217, v217, v210\n\t"
      "v_exp_f32 v213, v61\n\t"
      "s_nop 3\n\t"
      "v_mfma_f32_16x16x32_bf16 v[36:39], a[104:107], v[8:11], v[36:39]\n\t"
      "ds_read_b128 a[68:71], v220 offset:9728\n\t"
      "v_add_f32 v217, v217, v211\n\t"
      "v_mfma_f32_16x16x32_bf16 v[44:47], a[104:107], v[24:27], v[44:47]\n\t"
      "buffer_load_dwordx4 v224, %[sK], s40 offen offset:0 lds\n\t"
      "v_exp_f32 v214, v62\n\t"
      "v_cvt_pk_bf16_f32 v142, v212, v213\n\t"
      "v_add_f32 v217, v217, v212\n\t"
      "s_nop 3\n\t"
      "v_mfma_f32_16x16x32_bf16 v[36:39], a[108:111], v[12:15], v[36:39]\n\t"
      "ds_read_b128 a[72:75], v220 offset:12288\n\t"
      "v_exp_f32 v215, v63\n\t"
      "v_mfma_f32_16x16x32_bf16 v[44:47], a[108:111], v[28:31], v[44:47]\n\t"
      "buffer_load_dwordx4 v225, %[sK], s40 offen offset:1024 lds\n\t"
      "s_waitcnt lgkmcnt(5)\n\t"
      "v_add_f32 v217, v217, v213\n\t"
      "v_add_f32 v217, v217, v214\n\t"
      "v_cvt_pk_bf16_f32 v143, v214, v215\n\t"
      "s_nop 2\n\t"
      "v_mfma_f32_16x16x32_bf16 a[48:51], a[112:115], v[120:123], a[48:51]\n\t"
      "ds_read_b64_tr_b16 a[76:77], v221 offset:25088\n\t"
      "v_add_f32 v217, v217, v215\n\t"
      "v_mfma_f32_16x16x32_bf16 a[56:59], a[112:115], v[124:127], a[56:59]\n\t"
      "ds_read_b64_tr_b16 a[78:79], v221 offset:27136\n\t"
      "v_add_f32 v72, v216, v217\n\t"
      "v_cmp_nge_f32 vcc, %[sLIM], v72\n\t"
      "s_cbranch_vccnz .Lr7_%=\n\t"
      ".Lb7_%=:\n\t"
      "v_add_f32 v218, v218, v216\n\t"
      "v_add_f32 v219, v219, v217\n\t"
      "s_nop 2\n\t"
      "v_mfma_f32_16x16x32_bf16 v[48:51], a[116:119], v[0:3], v[64:67]\n\t"
      "ds_read_b128 a[80:83], v220 offset:12800\n\t"
      "v_exp_f32 v216, v32\n\t"
      "v_mfma_f32_16x16x32_bf16 v[56:59], a[116:119], v[16:19], v[68:71]\n\t"
      "buffer_load_dwordx4 v226, %[sK], s40 offen offset:2048 lds\n\t"
      "v_exp_f32 v209, v33\n\t"
      "v_exp_f32 v210, v34\n\t"
      "s_nop 2\n\t"
      "v_mfma_f32_16x16x32_bf16 v[48:51], a[120:123], v[4:7], v[48:51]\n\t"
      "ds_read_b128 a[84:87], v220 offset:13312\n\t"
      "v_cvt_pk_bf16_f32 v144, v216, v209\n\t"
      "v_mfma_f32_16x16x32_bf16 v[56:59], a[120:123], v[20:23], v[56:59]\n\t"
      "s_waitcnt lgkmcnt(5)\n\t"
      "v_exp_f32 v211, v35\n\t"
      "v_add_f32 v216, v216, v209\n\t"
      "v_exp_f32 v212, v36\n\t"
      "s_nop 2\n\t"
      "v_mfma_f32_16x16x32_bf16 a[52:55], a[124:127], v[120:123], a[52:55]\n\t"
      "ds_read_b128 a[88:91], v220 offset:13824\n\t"
      "v_cvt_pk_bf16_f32 v145, v210, v211\n\t"
      "v_mfma_f32_16x16x32_bf16 a[60:63], a[124:127], v[124:127], a[60:63]\n\t"
      "buffer_load_dwordx4 v227, %[sK], s40 offen offset:3072 lds\n\t"
      "v_add_f32 v216, v216, v210\n\t"
      "v_exp_f32 v213, v37\n\t"
      "v_add_f32 v216, v216, v211\n\t"
      "s_nop 3\n\t"
      "v_mfma_f32_16x16x32_bf16 v[48:51], a[64:67], v[8:11], v[48:51]\n\t"
      "ds_read_b64_tr_b16 a[92:93], v222 offset:25088\n\t"
      "v_exp_f32 v214, v38\n\t"
      "v_mfma_f32_16x16x32_bf16 v[56:59], a[64:67], v[24:27], v[56:59]\n\t"
      "ds_read_b64_tr_b16 a[94:95], v222 offset:27136\n\t"
      "v_cvt_pk_bf16_f32 v146, v212, v213\n\t"
      "v_add_f32 v216, v216, v212\n\t"
      "v_exp_f32 v215, v39\n\t"
      "v_add_f32 v216, v216, v213\n\t"
      "s_nop 1\n\t"
      "v_mfma_f32_16x16x32_bf16 v[48:51], a[68:71], v[12:15], v[48:51]\n\t"
      "v_mfma_f32_16x16x32_bf16 v[56:59], a[68:71], v[28:31], v[56:59]\n\t"
      "s_waitcnt vmcnt(14) lgkmcnt(4)\n\t"
      "s_barrier\n\t"
      "ds_read_b128 a[96:99], v220 offset:16384\n\t"
      "v_exp_f32 v217, v40\n\t"
      "v_cvt_pk_bf16_f32 v147, v214, v215\n\t"
      "s_nop 2\n\t"
      "v_mfma_f32_16x16x32_bf16 v[52:55], a[72:75], v[0:3], v[64:67]\n\t"
      "ds_read_b128 a[100:103], v220 offset:16896\n\t"
      "v_add_f32 v216, v216, v214\n\t"
      "v_mfma_f32_16x16x32_bf16 v[60:63], a[72:75], v[16:19], v[68:71]\n\t"
      "s_add_u32 m0, %[sMW], 49152\n\t"
      "s_mov_b32 s40, 98304\n\t"
      "v_exp_f32 v209, v41\n\t"
      "v_add_f32 v216, v216, v215\n\t"
      "s_nop 3\n\t"
      "v_mfma_f32_16x16x32_bf16 a[48:51], a[76:79], v[128:131], a[48:51]\n\t"
      "ds_read_b64_tr_b16 a[104:105], v221 offset:29184\n\t"
      "v_exp_f32 v210, v42\n\t"
      "v_mfma_f32_16x16x32_bf16 a[56:59], a[76:79], v[132:135], a[56:59]\n\t"
      "ds_read_b64_tr_b16 a[106:107], v221 offset:31232\n\t"
      "v_cvt_pk_bf16_f32 v148, v217, v209\n\t"
      "v_exp_f32 v211, v43\n\t"
      "v_add_f32 v217, v217, v209\n\t"
      "s_nop 2\n\t"
      "v_mfma_f32_16x16x32_bf16 v[52:55], a[80:83], v[4:7], v[52:55]\n\t"
      "ds_read_b128 a[108:111], v220 offset:17408\n\t"
      "v_exp_f32 v212, v44\n\t"
      "v_mfma_f32_16x16x32_bf16 v[60:63], a[80:83], v[20:23], v[60:63]\n\t"
      "buffer_load_dwordx4 v239, %[sVH], s40 offen offset:0 lds\n\t"
      "s_waitcnt lgkmcnt(5)\n\t"
      "v_cvt_pk_bf16_f32 v149, v210, v211\n\t"
      "v_add_f32 v217, v217, v210\n\t"
      "v_exp_f32 v213, v45\n\t"
      "s_nop 1\n\t"
      "v_mfma_f32_16x16x32_bf16 v[52:55], a[84:87], v[8:11], v[52:55]\n\t"
      "ds_read_b128 a[112:115], v220 offset:17920\n\t"
      "v_add_f32 v217, v217, v211\n\t"
      "v_mfma_f32_16x16x32_bf16 v[60:63], a[84:87], v[24:27], v[60:63]\n\t"
      "buffer_load_dwordx4 v235, %[sVH], s40 offen offset:1024 lds\n\t"
      "v_exp_f32 v214, v46\n\t"
      "v_cvt_pk_bf16_f32 v150, v212, v213\n\t"
      "v_add_f32 v217, v217, v212\n\t"
      "s_nop 3\n\t"
      "v_mfma_f32_16x16x32_bf16 v[52:55], a[88:91], v[12:15], v[52:55]\n\t"
      "ds_read_b128 a[116:119], v220 offset:20480\n\t"
      "v_exp_f32 v215, v47\n\t"
      "v_mfma_f32_16x16x32_bf16 v[60:63], a[88:91], v[28:31], v[60:63]\n\t"
      "v_add_f32 v217, v217, v213\n\t"
      "v_add_f32 v217, v217, v214\n\t"
      "v_cvt_pk_bf16_f32 v151, v214, v215\n\t"
      "v_add_f32 v217, v217, v215\n\t"
      "v_add_f32 v72, v216, v217\n\t"
      "s_nop 2\n\t"
      "v_mfma_f32_16x16x32_bf16 a[52:55], a[92:95], v[128:131], a[52:55]\n\t"
      "ds_read_b64_tr_b16 a[120:121], v222 offset:29184\n\t"
      "s_waitcnt lgkmcnt(4)\n\t"
      "v_mfma_f32_16x16x32_bf16 a[60:63], a[92:95], v[132:135], a[60:63]\n\t"
      "ds_read_b64_tr_b16 a[122:123], v222 offset:31232\n\t"
      "buffer_load_dwordx4 v246, %[sVH], s40 offen offset:2048 lds\n\t"
      "v_cmp_nge_f32 vcc, %[sLIM], v72\n\t"
      "s_cbranch_vccnz .Lr8_%=\n\t"
      ".Lb8_%=:\n\t"
      "v_add_f32 v218, v218, v216\n\t"
      "v_add_f32 v219, v219, v217\n\t"
      "s_nop 2\n\t"
      "v_mfma_f32_16x16x32_bf16 v[32:35], a[96:99], v[0:3], v[64:67]\n\t"
      "ds_read_b128 a[124:127], v220 offset:20992\n\t"
      "v_exp_f32 v216, v48\n\t"
      "v_mfma_f32_16x16x32_bf16 v[40:43], a[96:99], v[16:19], v[68:71]\n\t"
      "v_exp_f32 v209, v49\n\t"
      "v_exp_f32 v210, v50\n\t"
      "s_nop 3\n\t"
      "v_mfma_f32_16x16x32_bf16 v[32:35], a[100:103], v[4:7], v[32:35]\n\t"
      "ds_read_b128 a[64:67], v220 offset:21504\n\t"
      "v_cvt_pk_bf16_f32 v152, v216, v209\n\t"
      "v_mfma_f32_16x16x32_bf16 v[40:43], a[100:103], v[20:23], v[40:43]\n\t"
      "buffer_load_dwordx4 v247, %[sVH], s40 offen offset:3072 lds\n\t"
      "v_exp_f32 v211, v51\n\t"
      "v_add_f32 v216, v216, v209\n\t"
      "v_exp_f32 v212, v52\n\t"
      "s_nop 2\n\t"
      "v_mfma_f32_16x16x32_bf16 a[48:51], a[104:107], v[136:139], a[48:51]\n\t"
      "ds_read_b128 a[68:71], v220 offset:22016\n\t"
      "v_cvt_pk_bf16_f32 v153, v210, v211\n\t"
      "v_mfma_f32_16x16x32_bf16 a[56:59], a[104:107], v[140:143], a[56:59]\n\t"
      "s_waitcnt lgkmcnt(5)\n\t"
      "v_add_f32 v216, v216, v210\n\t"
      "v_exp_f32 v213, v53\n\t"
      "v_add_f32 v216, v216, v211\n\t"
      "s_nop 3\n\t"
      "v_mfma_f32_16x16x32_bf16 v[32:35], a[108:111], v[8:11], v[32:35]\n\t"
      "ds_read_b64_tr_b16 a[72:73], v221 offset:4096\n\t"
      "v_exp_f32 v214, v54\n\t"
      "v_mfma_f32_16x16x32_bf16 v[40:43], a[108:111], v[24:27], v[40:43]\n\t"
      "ds_read_b64_tr_b16 a[74:75], v221 offset:6144\n\t"
      "v_cvt_pk_bf16_f32 v154, v212, v213\n\t"
      "v_add_f32 v216, v216, v212\n\t"
      "v_exp_f32 v215, v55\n\t"
      "s_nop 2\n\t"
      "v_mfma_f32_16x16x32_bf16 v[32:35], a[112:115], v[12:15], v[32:35]\n\t"
      "ds_read_b128 a[76:79], v220 offset:24576\n\t"
      "v_add_f32 v216, v216, v213\n\t"
      "v_mfma_f32_16x16x32_bf16 v[40:43], a[112:115], v[28:31], v[40:43]\n\t"
      "s_add_u32 m0, %[sMW], 65536\n\t"
      "v_exp_f32 v217, v56\n\t"
      "v_cvt_pk_bf16_f32 v155, v214, v215\n\t"
      "v_add_f32 v216, v216, v214\n\t"
      "s_nop 3\n\t"
      "v_mfma_f32_16x16x32_bf16 v[36:39], a[116:119], v[0:3], v[64:67]\n\t"
      "ds_read_b128 a[80:83], v220 offset:25088\n\t"
      "v_exp_f32 v209, v57\n\t"
      "v_mfma_f32_16x16x32_bf16 v[44:47], a[116:119], v[16:19], v[68:71]\n\t"
      "s_waitcnt lgkmcnt(5)\n\t"
      "v_add_f32 v216, v216, v215\n\t"
      "v_exp_f32 v210, v58\n\t"
      "s_nop 3\n\t"
      "v_mfma_f32_16x16x32_bf16 a[52:55], a[120:123], v[136:139], a[52:55]\n\t"
      "ds_read_b64_tr_b16 a[84:85], v222 offset:4096\n\t"
      "v_cvt_pk_bf16_f32 v156, v217, v209\n\t"
      "v_mfma_f32_16x16x32_bf16 a[60:63], a[120:123], v[140:143], a[60:63]\n\t"
      "ds_read_b64_tr_b16 a[86:87], v222 offset:6144\n\t"
      "buffer_load_dwordx4 v239, %[sVL], %[sV01] offen offset:0 lds\n\t"
      "v_exp_f32 v211, v59\n\t"
      "v_add_f32 v217, v217, v209\n\t"
      "s_nop 3\n\t"
      "v_mfma_f32_16x16x32_bf16 v[36:39], a[124:127], v[4:7], v[36:39]\n\t"
      "ds_read_b128 a[88:91], v220 offset:25600\n\t"
      "v_exp_f32 v212, v60\n\t"
      "v_mfma_f32_16x16x32_bf16 v[44:47], a[124:127], v[20:23], v[44:47]\n\t"
      "buffer_load_dwordx4 v235, %[sVL], %[sV01] offen offset:1024 lds\n\t"
      "v_cvt_pk_bf16_f32 v157, v210, v211\n\t"
      "v_add_f32 v217, v217, v210\n\t"
      "v_exp_f32 v213, v61\n\t"
      "s_nop 2\n\t"
      "v_mfma_f32_16x16x32_bf16 v[36:39], a[64:67], v[8:11], v[36:39]\n\t"
      "ds_read_b128 a[92:95], v220 offset:26112\n\t"
      "v_add_f32 v217, v217, v211\n\t"
      "v_mfma_f32_16x16x32_bf16 v[44:47], a[64:67], v[24:27], v[44:47]\n\t"
      "s_waitcnt lgkmcnt(5)\n\t"
      "v_exp_f32 v214, v62\n\t"
      "v_cvt_pk_bf16_f32 v158, v212, v213\n\t"
      "v_add_f32 v217, v217, v212\n\t"
      "s_nop 3\n\t"
      "v_mfma_f32_16x16x32_bf16 v[36:39], a[68:71], v[12:15], v[36:39]\n\t"
      "ds_read_b128 a[96:99], v220 offset:28672\n\t"
      "v_exp_f32 v215, v63\n\t"
      "v_mfma_f32_16x16x32_bf16 v[44:47], a[68:71], v[28:31], v[44:47]\n\t"
      "buffer_load_dwordx4 v246, %[sVL], %[sV01] offen offset:2048 lds\n\t"
      "v_add_f32 v217, v217, v213\n\t"
      "v_add_f32 v217, v217, v214\n\t"
      "v_cvt_pk_bf16_f32 v159, v214, v215\n\t"
      "v_add_f32 v217, v217, v215\n\t"
      "s_nop 2\n\t"
      "v_mfma_f32_16x16x32_bf16 a[32:35], a[72:75], v[88:91], a[32:35]\n\t"
      "ds_read_b64_tr_b16 a[100:101], v221 offset:8192\n\t"
      "v_add_f32 v72, v216, v217\n\t"
      "v_mfma_f32_16x16x32_bf16 a[40:43], a[72:75], v[92:95], a[40:43]\n\t"
      "ds_read_b64_tr_b16 a[102:103], v221 offset:10240\n\t"
      "v_cmp_nge_f32 vcc, %[sLIM], v72\n\t"
      "s_cbranch_vccnz .Lr9_%=\n\t"
      ".Lb9_%=:\n\t"
      "v_add_f32 v218, v218, v216\n\t"
      "v_add_f32 v219, v219, v217\n\t"
      "s_nop 3\n\t"
      "v_mfma_f32_16x16x32_bf16 v[48:51], a[76:79], v[0:3], v[64:67]\n\t"
      "ds_read_b128 a[104:107], v220 offset:29184\n\t"
      "v_exp_f32 v216, v32\n\t"
      "v_mfma_f32_16x16x32_bf16 v[56:59], a[76:79], v[16:19], v[68:71]\n\t"
      "buffer_load_dwordx4 v247, %[sVL], %[sV01] offen offset:3072 lds\n\t"
      "s_waitcnt lgkmcnt(5)\n\t"
      "v_exp_f32 v209, v33\n\t"
      "v_exp_f32 v210, v34\n\t"
      "s_waitcnt vmcnt(16)\n\t"
      "s_mov_b64 exec, 14\n\t"
      "global_store_dword v231, v230, %[sFL] sc1\n\t"
      "s_mov_b64 exec, -1\n\t"
      "s_nop 1\n\t"
      "v_mfma_f32_16x16x32_bf16 v[48:51], a[80:83], v[4:7], v[48:51]\n\t"
      "ds_read_b128 a[108:111], v220 offset:29696\n\t"
      "v_cvt_pk_bf16_f32 v160, v216, v209\n\t"
      "v_mfma_f32_16x16x32_bf16 v[56:59], a[80:83], v[20:23], v[56:59]\n\t"
      "v_exp_f32 v211, v35\n\t"
      "v_add_f32 v216, v216, v209\n\t"
      "v_exp_f32 v212, v36\n\t"
      "s_nop 3\n\t"
      "v_mfma_f32_16x16x32_bf16 a[36:39], a[84:87], v[88:91], a[36:39]\n\t"
      "ds_read_b128 a[112:115], v220 offset:30208\n\t"
      "v_cvt_pk_bf16_f32 v161, v210, v211\n\t"
      "v_mfma_f32_16x16x32_bf16 a[44:47], a[84:87], v[92:95], a[44:47]\n\t"
      "v_add_f32 v216, v216, v210\n\t"
      "v_exp_f32 v213, v37\n\t"
      "v_add_f32 v216, v216, v211\n\t"
      "v_exp_f32 v214, v38\n\t"
      "s_nop 2\n\t"
      "v_mfma_f32_16x16x32_bf16 v[48:51], a[88:91], v[8:11], v[48:51]\n\t"
      "ds_read_b64_tr_b16 a[116:117], v222 offset:8192\n\t"
      "v_cvt_pk_bf16_f32 v162, v212, v213\n\t"
      "v_mfma_f32_16x16x32_bf16 v[56:59], a[88:91], v[24:27], v[56:59]\n\t"
      "ds_read_b64_tr_b16 a[118:119], v222 offset:10240\n\t"
      "s_add_u32 m0, %[sMW], 81920\n\t"
      "s_mov_b32 s40, 32768\n\t"
      "s_waitcnt lgkmcnt(5)\n\t"
      "v_add_f32 v216, v216, v212\n\t"
      "v_exp_f32 v215, v39\n\t"
      "s_nop 1\n\t"
      "v_mfma_f32_16x16x32_bf16 v[48:51], a[92:95], v[12:15], v[48:51]\n\t"
      "v_mfma_f32_16x16x32_bf16 v[56:59], a[92:95], v[28:31], v[56:59]\n\t"
      "s_waitcnt vmcnt(13)\n\t"
      "s_barrier\n\t"
      "ds_read_b128 a[120:123], v220 offset:32768\n\t"
      "v_add_f32 v216, v216, v213\n\t"
      "v_exp_f32 v217, v40\n\t"
      "s_nop 2\n\t"
      "v_mfma_f32_16x16x32_bf16 v[52:55], a[96:99], v[0:3], v[64:67]\n\t"
      "ds_read_b128 a[124:127], v220 offset:33280\n\t"
      "v_cvt_pk_bf16_f32 v163, v214, v215\n\t"
      "v_mfma_f32_16x16x32_bf16 v[60:63], a[96:99], v[16:19], v[68:71]\n\t"
      "buffer_load_dwordx4 v239, %[sVL], s40 offen offset:0 lds\n\t"
      "v_add_f32 v216, v216, v214\n\t"
      "v_exp_f32 v209, v41\n\t"
      "v_add_f32 v216, v216, v215\n\t"
      "s_nop 3\n\t"
      "v_mfma_f32_16x16x32_bf16 a[32:35], a[100:103], v[96:99], a[32:35]\n\t"
      "ds_read_b64_tr_b16 a[64:65], v221 offset:33280\n\t"
      "v_exp_f32 v210, v42\n\t"
      "v_mfma_f32_16x16x32_bf16 a[40:43], a[100:103], v[100:103], a[40:43]\n\t"
      "ds_read_b64_tr_b16 a[66:67], v221 offset:35328\n\t"
      "buffer_load_dwordx4 v235, %[sVL], s40 offen offset:1024 lds\n\t"
      "s_waitcnt lgkmcnt(6)\n\t"
      "v_cvt_pk_bf16_f32 v164, v217, v209\n\t"
      "v_exp_f32 v211, v43\n\t"
      "s_nop 1\n\t"
      "v_mfma_f32_16x16x32_bf16 v[52:55], a[104:107], v[4:7], v[52:55]\n\t"
      "ds_read_b128 a[68:71], v220 offset:33792\n\t"
      "v_add_f32 v217, v217, v209\n\t"
      "v_mfma_f32_16x16x32_bf16 v[60:63], a[104:107], v[20:23], v[60:63]\n\t"
      "v_exp_f32 v212, v44\n\t"
      "v_cvt_pk_bf16_f32 v165, v210, v211\n\t"
      "v_add_f32 v217, v217, v210\n\t"
      "v_exp_f32 v213, v45\n\t"
      "s_nop 2\n\t"
      "v_mfma_f32_16x16x32_bf16 v[52:55], a[108:111], v[8:11], v[52:55]\n\t"
      "ds_read_b128 a[72:75], v220 offset:34304\n\t"
      "v_add_f32 v217, v217, v211\n\t"
      "v_mfma_f32_16x16x32_bf16 v[60:63], a[108:111], v[24:27], v[60:63]\n\t"
      "buffer_load_dwordx4 v246, %[sVL], s40 offen offset:2048 lds\n\t"
      "v_exp_f32 v214, v46\n\t"
      "v_cvt_pk_bf16_f32 v166, v212, v213\n\t"
      "v_add_f32 v217, v217, v212\n\t"
      "s_nop 3\n\t"
      "v_mfma_f32_16x16x32_bf16 v[52:55], a[112:115], v[12:15], v[52:55]\n\t"
      "ds_read_b128 a[76:79], v220 offset:36864\n\t"
      "v_exp_f32 v215, v47\n\t"
      "v_mfma_f32_16x16x32_bf16 v[60:63], a[112:115], v[28:31], v[60:63]\n\t"
      "s_waitcnt lgkmcnt(5)\n\t"
      "v_add_f32 v217, v217, v213\n\t"
      "v_add_f32 v217, v217, v214\n\t"
      "v_cvt_pk_bf16_f32 v167, v214, v215\n\t"
      "v_add_f32 v217, v217, v215\n\t"
      "s_nop 2\n\t"
      "v_mfma_f32_16x16x32_bf16 a[36:39], a[116:119], v[96:99], a[36:39]\n\t"
      "ds_read_b64_tr_b16 a[80:81], v222 offset:33280\n\t"
      "v_add_f32 v72, v216, v217\n\t"
      "v_mfma_f32_16x16x32_bf16 a[44:47], a[116:119], v[100:103], a[44:47]\n\t"
      "ds_read_b64_tr_b16 a[82:83], v222 offset:35328\n\t"
      "buffer_load_dwordx4 v247, %[sVL], s40 offen offset:3072 lds\n\t"
      "v_cmp_nge_f32 vcc, %[sLIM], v72\n\t"
      "s_cbranch_vccnz .Lr10_%=\n\t"
      ".Lb10_%=:\n\t"
      "v_add_f32 v218, v218, v216\n\t"
      "v_add_f32 v219, v219, v217\n\t"
      "s_nop 2\n\t"
      "v_mfma_f32_16x16x32_bf16 v[32:35], a[120:123], v[0:3], v[64:67]\n\t"
      "ds_read_b128 a[84:87], v220 offset:37376\n\t"
      "v_exp_f32 v216, v48\n\t"
      "v_mfma_f32_16x16x32_bf16 v[40:43], a[120:123], v[16:19], v[68:71]\n\t"
      "v_exp_f32 v209, v49\n\t"
      "v_exp_f32 v210, v50\n\t"
      "v_cvt_pk_bf16_f32 v168, v216, v209\n\t"
      "s_nop 2\n\t"
      "v_mfma_f32_16x16x32_bf16 v[32:35], a[124:127], v[4:7], v[32:35]\n\t"
      "ds_read_b128 a[88:91], v220 offset:37888\n\t"
      "v_exp_f32 v211, v51\n\t"
      "v_mfma_f32_16x16x32_bf16 v[40:43], a[124:127], v[20:23], v[40:43]\n\t"
      "s_waitcnt lgkmcnt(5)\n\t"
      "v_add_f32 v216, v216, v209\n\t"
      "v_exp_f32 v212, v52\n\t"
      "s_nop 3\n\t"
      "v_mfma_f32_16x16x32_bf16 a[48:51], a[64:67], v[144:147], a[48:51]\n\t"
      "ds_read_b128 a[92:95], v220 offset:38400\n\t"
      "v_cvt_pk_bf16_f32 v169, v210, v211\n\t"
      "v_mfma_f32_16x16x32_bf16 a[56:59], a[64:67], v[148:151], a[56:59]\n\t"
      "s_add_u32 m0, %[sMW], 98304\n\t"
      "s_mov_b32 s40, 65536\n\t"
      "v_add_f32 v216, v216, v210\n\t"
      "v_exp_f32 v213, v53\n\t"
      "v_add_f32 v216, v216, v211\n\t"
      "s_nop 2\n\t"
      "v_mfma_f32_16x16x32_bf16 v[32:35], a[68:71], v[8:11], v[32:35]\n\t"
      "ds_read_b64_tr_b16 a[96:97], v221 offset:37376\n\t"
      "v_exp_f32 v214, v54\n\t"
      "v_mfma_f32_16x16x32_bf16 v[40:43], a[68:71], v[24:27], v[40:43]\n\t"
      "ds_read_b64_tr_b16 a[98:99], v221 offset:39424\n\t"
      "v_cvt_pk_bf16_f32 v170, v212, v213\n\t"
      "v_add_f32 v216, v216, v212\n\t"
      "v_exp_f32 v215, v55\n\t"
      "s_nop 2\n\t"
      "v_mfma_f32_16x16x32_bf16 v[32:35], a[72:75], v[12:15], v[32:35]\n\t"
      "ds_read_b128 a[100:103], v220 offset:40960\n\t"
      "v_add_f32 v216, v216, v213\n\t"
      "v_mfma_f32_16x16x32_bf16 v[40:43], a[72:75], v[28:31], v[40:43]\n\t"
      "buffer_load_dwordx4 v239, %[sVL], s40 offen offset:0 lds\n\t"
      "s_waitcnt lgkmcnt(5)\n\t"
      "v_exp_f32 v217, v56\n\t"
      "v_cvt_pk_bf16_f32 v171, v214, v215\n\t"
      "s_nop 3\n\t"
      "v_mfma_f32_16x16x32_bf16 v[36:39], a[76:79], v[0:3], v[64:67]\n\t"
      "ds_read_b128 a[104:107], v220 offset:41472\n\t"
      "v_add_f32 v216, v216, v214\n\t"
      "v_mfma_f32_16x16x32_bf16 v[44:47], a[76:79], v[16:19], v[68:71]\n\t"
      "buffer_load_dwordx4 v235, %[sVL], s40 offen offset:1024 lds\n\t"
      "v_exp_f32 v209, v57\n\t"
      "v_add_f32 v216, v216, v215\n\t"
      "v_exp_f32 v210, v58\n\t"
      "s_nop 2\n\t"
      "v_mfma_f32_16x16x32_bf16 a[52:55], a[80:83], v[144:147], a[52:55]\n\t"
      "ds_read_b64_tr_b16 a[108:109], v222 offset:37376\n\t"
      "v_cvt_pk_bf16_f32 v172, v217, v209\n\t"
      "v_mfma_f32_16x16x32_bf16 a[60:63], a[80:83], v[148:151], a[60:63]\n\t"
      "ds_read_b64_tr_b16 a[110:111], v222 offset:39424\n\t"
      "v_exp_f32 v211, v59\n\t"
      "v_add_f32 v217, v217, v209\n\t"
      "s_nop 4\n\t"
      "v_mfma_f32_16x16x32_bf16 v[36:39], a[84:87], v[4:7], v[36:39]\n\t"
      "ds_read_b128 a[112:115], v220 offset:41984\n\t"
      "v_exp_f32 v212, v60\n\t"
      "v_mfma_f32_16x16x32_bf16 v[44:47], a[84:87], v[20:23], v[44:47]\n\t"
      "buffer_load_dwordx4 v246, %[sVL], s40 offen offset:2048 lds\n\t"
      "s_waitcnt lgkmcnt(5)\n\t"
      "v_cvt_pk_bf16_f32 v173, v210, v211\n\t"
      "v_add_f32 v217, v217, v210\n\t"
      "v_exp_f32 v213, v61\n\t"
      "s_nop 1\n\t"
      "v_mfma_f32_16x16x32_bf16 v[36:39], a[88:91], v[8:11], v[36:39]\n\t"
      "ds_read_b128 a[116:119], v220 offset:42496\n\t"
      "v_add_f32 v217, v217, v211\n\t"
      "v_mfma_f32_16x16x32_bf16 v[44:47], a[88:91], v[24:27], v[44:47]\n\t"
      "v_exp_f32 v214, v62\n\t"
      "v_cvt_pk_bf16_f32 v174, v212, v213\n\t"
      "v_add_f32 v217, v217, v212\n\t"
      "s_nop 4\n\t"
      "v_mfma_f32_16x16x32_bf16 v[36:39], a[92:95], v[12:15], v[36:39]\n\t"
      "ds_read_b128 a[120:123], v220 offset:45056\n\t"
      "v_exp_f32 v215, v63\n\t"
      "v_mfma_f32_16x16x32_bf16 v[44:47], a[92:95], v[28:31], v[44:47]\n\t"
      "buffer_load_dwordx4 v247, %[sVL], s40 offen offset:3072 lds\n\t"
      "v_add_f32 v217, v217, v213\n\t"
      "v_add_f32 v217, v217, v214\n\t"
      "v_cvt_pk_bf16_f32 v175, v214, v215\n\t"
      "v_add_f32 v217, v217, v215\n\t"
      "s_nop 2\n\t"
      "v_mfma_f32_16x16x32_bf16 a[48:51], a[96:99], v[152:155], a[48:51]\n\t"
      "ds_read_b64_tr_b16 a[124:125], v221 offset:41472\n\t"
      "v_add_f32 v72, v216, v217\n\t"
      "v_mfma_f32_16x16x32_bf16 a[56:59], a[96:99], v[156:159], a[56:59]\n\t"
      "ds_read_b64_tr_b16 a[126:127], v221 offset:43520\n\t"
      "s_waitcnt lgkmcnt(5)\n\t"
      "v_cmp_nge_f32 vcc, %[sLIM], v72\n\t"
      "s_cbranch_vccnz .Lr11_%=\n\t"
      ".Lb11_%=:\n\t"
      "v_add_f32 v218, v218, v216\n\t"
      "v_add_f32 v219, v219, v217\n\t"
      "s_nop 2\n\t"
      "v_mfma_f32_16x16x32_bf16 v[48:51], a[100:103], v[0:3], v[64:67]\n\t"
      "ds_read_b128 a[64:67], v220 offset:45568\n\t"
      "v_exp_f32 v216, v32\n\t"
      "v_mfma_f32_16x16x32_bf16 v[56:59], a[100:103], v[16:19], v[68:71]\n\t"
      "global_load_dword v234, v232, %[sFL] sc1\n\t"
      "v_exp_f32 v209, v33\n\t"
      "v_exp_f32 v210, v34\n\t"
      "v_cvt_pk_bf16_f32 v176, v216, v209\n\t"
      "s_nop 0\n\t"
      "v_mfma_f32_16x16x32_bf16 v[48:51], a[104:107], v[4:7], v[48:51]\n\t"
      "v_mfma_f32_16x16x32_bf16 v[56:59], a[104:107], v[20:23], v[56:59]\n\t"
      "s_waitcnt vmcnt(14)\n\t"
      "s_barrier\n\t"
      "ds_read_b128 a[68:71], v220 offset:46080\n\t"
      "s_add_u32 m0, %[sMW], 114688\n\t"
      "s_mov_b32 s40, 98304\n\t"
      "s_nop 2\n\t"
      "v_mfma_f32_16x16x32_bf16 a[52:55], a[108:111], v[152:155], a[52:55]\n\t"
      "ds_read_b128 a[72:75], v220 offset:46592\n\t"
      "v_exp_f32 v211, v35\n\t"
      "v_mfma_f32_16x16x32_bf16 a[60:63], a[108:111], v[156:159], a[60:63]\n\t"
      "s_waitcnt lgkmcnt(5)\n\t"
      "v_add_f32 v216, v216, v209\n\t"
      "v_exp_f32 v212, v36\n\t"
      "v_cvt_pk_bf16_f32 v177, v210, v211\n\t"
      "s_nop 1\n\t"
      "v_mfma_f32_16x16x32_bf16 v[48:51], a[112:115], v[8:11], v[48:51]\n\t"
      "ds_read_b64_tr_b16 a[76:77], v222 offset:41472\n\t"
      "v_add_f32 v216, v216, v210\n\t"
      "v_mfma_f32_16x16x32_bf16 v[56:59], a[112:115], v[24:27], v[56:59]\n\t"
      "ds_read_b64_tr_b16 a[78:79], v222 offset:43520\n\t"
      "buffer_load_dwordx4 v239, %[sVL], s40 offen offset:0 lds\n\t"
      "v_exp_f32 v213, v37\n\t"
      "v_add_f32 v216, v216, v211\n\t"
      "v_exp_f32 v214, v38\n\t"
      "s_nop 0\n\t"
      "v_mfma_f32_16x16x32_bf16 v[48:51], a[116:119], v[12:15], v[48:51]\n\t"
      "ds_read_b128 a[80:83], v220 offset:49152\n\t"
      "v_cvt_pk_bf16_f32 v178, v212, v213\n\t"
      "v_mfma_f32_16x16x32_bf16 v[56:59], a[116:119], v[28:31], v[56:59]\n\t"
      "buffer_load_dwordx4 v235, %[sVL], s40 offen offset:1024 lds\n\t"
      "v_add_f32 v216, v216, v212\n\t"
      "v_exp_f32 v215, v39\n\t"
      "v_add_f32 v216, v216, v213\n\t"
      "s_nop 2\n\t"
      "v_mfma_f32_16x16x32_bf16 v[52:55], a[120:123], v[0:3], v[64:67]\n\t"
      "ds_read_b128 a[84:87], v220 offset:49664\n\t"
      "v_exp_f32 v217, v40\n\t"
      "v_mfma_f32_16x16x32_bf16 v[60:63], a[120:123], v[16:19], v[68:71]\n\t"
      "s_waitcnt lgkmcnt(5)\n\t"
      "v_cvt_pk_bf16_f32 v179, v214, v215\n\t"
      "v_add_f32 v216, v216, v214\n\t"
      "v_exp_f32 v209, v41\n\t"
      "s_nop 1\n\t"
      "v_mfma_f32_16x16x32_bf16 a[48:51], a[124:127], v[160:163], a[48:51]\n\t"
      "ds_read_b64_tr_b16 a[88:89], v221 offset:45568\n\t"
      "v_add_f32 v216, v216, v215\n\t"
      "v_mfma_f32_16x16x32_bf16 a[56:59], a[124:127], v[164:167], a[56:59]\n\t"
      "ds_read_b64_tr_b16 a[90:91], v221 offset:47616\n\t"
      "buffer_load_dwordx4 v246, %[sVL], s40 offen offset:2048 lds\n\t"
      "v_exp_f32 v210, v42\n\t"
      "v_cvt_pk_bf16_f32 v180, v217, v209\n\t"
      "s_nop 2\n\t"
      "v_mfma_f32_16x16x32_bf16 v[52:55], a[64:67], v[4:7], v[52:55]\n\t"
      "ds_read_b128 a[92:95], v220 offset:50176\n\t"
      "v_exp_f32 v211, v43\n\t"
      "v_mfma_f32_16x16x32_bf16 v[60:63], a[64:67], v[20:23], v[60:63]\n\t"
      "v_add_f32 v217, v217, v209\n\t"
      "v_exp_f32 v212, v44\n\t"
      "v_cvt_pk_bf16_f32 v181, v210, v211\n\t"
      "v_add_f32 v217, v217, v210\n\t"
      "s_nop 1\n\t"
      "v_mfma_f32_16x16x32_bf16 v[52:55], a[68:71], v[8:11], v[52:55]\n\t"
      "ds_read_b128 a[96:99], v220 offset:50688\n\t"
      "v_exp_f32 v213, v45\n\t"
      "v_mfma_f32_16x16x32_bf16 v[60:63], a[68:71], v[24:27], v[60:63]\n\t"
      "buffer_load_dwordx4 v247, %[sVL], s40 offen offset:3072 lds\n\t"
      "s_waitcnt lgkmcnt(5)\n\t"
      "v_add_f32 v217, v217, v211\n\t"
      "v_exp_f32 v214, v46\n\t"
      "s_nop 1\n\t"
      "v_mfma_f32_16x16x32_bf16 v[52:55], a[72:75], v[12:15], v[52:55]\n\t"
      "ds_read_b128 a[100:103], v220 offset:53248\n\t"
      "v_cvt_pk_bf16_f32 v182, v212, v213\n\t"
      "v_mfma_f32_16x16x32_bf16 v[60:63], a[72:75], v[28:31], v[60:63]\n\t"
      "v_add_f32 v217, v217, v212\n\t"
      "v_exp_f32 v215, v47\n\t"
      "v_add_f32 v217, v217, v213\n\t"
      "v_add_f32 v217, v217, v214\n\t"
      "v_cvt_pk_bf16_f32 v183, v214, v215\n\t"
      "s_nop 1\n\t"
      "v_mfma_f32_16x16x32_bf16 a[52:55], a[76:79], v[160:163], a[52:55]\n\t"
      "ds_read_b64_tr_b16 a[104:105], v222 offset:45568\n\t"
      "v_add_f32 v217, v217, v215\n\t"
      "v_mfma_f32_16x16x32_bf16 a[60:63], a[76:79], v[164:167], a[60:63]\n\t"
      "ds_read_b64_tr_b16 a[106:107], v222 offset:47616\n\t"
      "v_add_f32 v72, v216, v217\n\t"
      "v_cmp_nge_f32 vcc, %[sLIM], v72\n\t"
      "s_cbranch_vccnz .Lr12_%=\n\t"
      ".Lb12_%=:\n\t"
      "v_add_f32 v218, v218, v216\n\t"
      "v_add_f32 v219, v219, v217\n\t"
      "s_nop 1\n\t"
      "v_mfma_f32_16x16x32_bf16 v[32:35], a[80:83], v[0:3], v[64:67]\n\t"
      "ds_read_b128 a[108:111], v220 offset:53760\n\t"
      "v_exp_f32 v216, v48\n\t"
      "v_mfma_f32_16x16x32_bf16 v[40:43], a[80:83], v[16:19], v[68:71]\n\t"
      "s_waitcnt lgkmcnt(5)\n\t"
      "v_exp_f32 v209, v49\n\t"
      "v_exp_f32 v210, v50\n\t"
      "v_mfma_f32_16x16x32_bf16 v[32:35], a[84:87], v[4:7], v[32:35]\n\t"
      "ds_read_b128 a[112:115], v220 offset:54272\n\t"
      "v_cvt_pk_bf16_f32 v184, v216, v209\n\t"
      "v_mfma_f32_16x16x32_bf16 v[40:43], a[84:87], v[20:23], v[40:43]\n\t"
      "v_exp_f32 v211, v51\n\t"
      "v_add_f32 v216, v216, v209\n\t"
      "v_exp_f32 v212, v52\n\t"
      "v_mfma_f32_16x16x32_bf16 a[48:51], a[88:91], v[168:171], a[48:51]\n\t"
      "ds_read_b128 a[116:119], v220 offset:54784\n\t"
      "v_cvt_pk_bf16_f32 v185, v210, v211\n\t"
      "v_mfma_f32_16x16x32_bf16 a[56:59], a[88:91], v[172:175], a[56:59]\n\t"
      "v_add_f32 v216, v216, v210\n\t"
      "v_exp_f32 v213, v53\n\t"
      "v_add_f32 v216, v216, v211\n\t"
      "v_mfma_f32_16x16x32_bf16 v[32:35], a[92:95], v[8:11], v[32:35]\n\t"
      "ds_read_b64_tr_b16 a[120:121], v221 offset:12288\n\t"
      "v_exp_f32 v214, v54\n\t"
      "v_mfma_f32_16x16x32_bf16 v[40:43], a[92:95], v[24:27], v[40:43]\n\t"
      "ds_read_b64_tr_b16 a[122:123], v221 offset:14336\n\t"
      "s_waitcnt lgkmcnt(5)\n\t"
      "v_cvt_pk_bf16_f32 v186, v212, v213\n\t"
      "v_add_f32 v216, v216, v212\n\t"
      "v_mfma_f32_16x16x32_bf16 v[32:35], a[96:99], v[12:15], v[32:35]\n\t"
      "ds_read_b128 a[124:127], v220 offset:57344\n\t"
      "v_exp_f32 v215, v55\n\t"
      "v_mfma_f32_16x16x32_bf16 v[40:43], a[96:99], v[28:31], v[40:43]\n\t"
      "v_add_f32 v216, v216, v213\n\t"
      "v_exp_f32 v217, v56\n\t"
      "v_cvt_pk_bf16_f32 v187, v214, v215\n\t"
      "v_mfma_f32_16x16x32_bf16 v[36:39], a[100:103], v[0:3], v[64:67]\n\t"
      "ds_read_b128 a[64:67], v220 offset:57856\n\t"
      "v_add_f32 v216, v216, v214\n\t"
      "v_mfma_f32_16x16x32_bf16 v[44:47], a[100:103], v[16:19], v[68:71]\n\t"
      "v_exp_f32 v209, v57\n\t"
      "v_add_f32 v216, v216, v215\n\t"
      "v_exp_f32 v210, v58\n\t"
      "v_mfma_f32_16x16x32_bf16 a[52:55], a[104:107], v[168:171], a[52:55]\n\t"
      "ds_read_b64_tr_b16 a[68:69], v221 offset:49664\n\t"
      "v_cvt_pk_bf16_f32 v188, v217, v209\n\t"
      "v_mfma_f32_16x16x32_bf16 a[60:63], a[104:107], v[172:175], a[60:63]\n\t"
      "ds_read_b64_tr_b16 a[70:71], v221 offset:51712\n\t"
      "s_waitcnt lgkmcnt(6)\n\t"
      "v_exp_f32 v211, v59\n\t"
      "v_add_f32 v217, v217, v209\n\t"
      "v_mfma_f32_16x16x32_bf16 v[36:39], a[108:111], v[4:7], v[36:39]\n\t"
      "ds_read_b128 a[72:75], v220 offset:58368\n\t"
      "v_exp_f32 v212, v60\n\t"
      "v_mfma_f32_16x16x32_bf16 v[44:47], a[108:111], v[20:23], v[44:47]\n\t"
      "v_cvt_pk_bf16_f32 v189, v210, v211\n\t"
      "v_add_f32 v217, v217, v210\n\t"
      "v_exp_f32 v213, v61\n\t"
      "v_mfma_f32_16x16x32_bf16 v[36:39], a[112:115], v[8:11], v[36:39]\n\t"
      "ds_read_b128 a[76:79], v220 offset:58880\n\t"
      "v_add_f32 v217, v217, v211\n\t"
      "v_mfma_f32_16x16x32_bf16 v[44:47], a[112:115], v[24:27], v[44:47]\n\t"
      "v_exp_f32 v214, v62\n\t"
      "v_cvt_pk_bf16_f32 v190, v212, v213\n\t"
      "v_add_f32 v217, v217, v212\n\t"
      "v_exp_f32 v215, v63\n\t"
      "v_mfma_f32_16x16x32_bf16 v[36:39], a[116:119], v[12:15], v[36:39]\n\t"
      "ds_read_b128 a[80:83], v220 offset:61440\n\t"
      "v_add_f32 v217, v217, v213\n\t"
      "v_mfma_f32_16x16x32_bf16 v[44:47], a[116:119], v[28:31], v[44:47]\n\t"
      "s_waitcnt lgkmcnt(5)\n\t"
      "v_add_f32 v217, v217, v214\n\t"
      "v_cvt_pk_bf16_f32 v191, v214, v215\n\t"
      "v_add_f32 v217, v217, v215\n\t"
      "v_mfma_f32_16x16x32_bf16 a[32:35], a[120:123], v[104:107], a[32:35]\n\t"
      "ds_read_b64_tr_b16 a[84:85], v222 offset:49664\n\t"
      "v_add_f32 v72, v216, v217\n\t"
      "v_mfma_f32_16x16x32_bf16 a[40:43], a[120:123], v[108:111], a[40:43]\n\t"
      "ds_read_b64_tr_b16 a[86:87], v222 offset:51712\n\t"
      "v_cmp_nge_f32 vcc, %[sLIM], v72\n\t"
      "s_cbranch_vccnz .Lr13_%=\n\t"
      ".Lb13_%=:\n\t"
      "v_add_f32 v218, v218, v216\n\t"
      "v_add_f32 v219, v219, v217\n\t"
      "v_mfma_f32_16x16x32_bf16 v[48:51], a[124:127], v[0:3], v[64:67]\n\t"
      "ds_read_b128 a[88:91], v220 offset:61952\n\t"
      "v_exp_f32 v216, v32\n\t"
      "v_mfma_f32_16x16x32_bf16 v[56:59], a[124:127], v[16:19], v[68:71]\n\t"
      "v_exp_f32 v209, v33\n\t"
      "v_exp_f32 v210, v34\n\t"
      "v_mfma_f32_16x16x32_bf16 v[48:51], a[64:67], v[4:7], v[48:51]\n\t"
      "ds_read_b128 a[92:95], v220 offset:62464\n\t"
      "v_cvt_pk_bf16_f32 v192, v216, v209\n\t"
      "v_mfma_f32_16x16x32_bf16 v[56:59], a[64:67], v[20:23], v[56:59]\n\t"
      "s_waitcnt lgkmcnt(5)\n\t"
      "v_exp_f32 v211, v35\n\t"
      "v_add_f32 v216, v216, v209\n\t"
      "v_exp_f32 v212, v36\n\t"
      "v_mfma_f32_16x16x32_bf16 a[48:51], a[68:71], v[176:179], a[48:51]\n\t"
      "ds_read_b128 a[96:99], v220 offset:62976\n\t"
      "v_cvt_pk_bf16_f32 v193, v210, v211\n\t"
      "v_mfma_f32_16x16x32_bf16 a[56:59], a[68:71], v[180:183], a[56:59]\n\t"
      "v_add_f32 v216, v216, v210\n\t"
      "v_exp_f32 v213, v37\n\t"
      "v_add_f32 v216, v216, v211\n\t"
      "v_exp_f32 v214, v38\n\t"
      "v_mfma_f32_16x16x32_bf16 v[48:51], a[72:75], v[8:11], v[48:51]\n\t"
      "ds_read_b64_tr_b16 a[100:101], v221 offset:53760\n\t"
      "v_cvt_pk_bf16_f32 v194, v212, v213\n\t"
      "v_mfma_f32_16x16x32_bf16 v[56:59], a[72:75], v[24:27], v[56:59]\n\t"
      "ds_read_b64_tr_b16 a[102:103], v221 offset:55808\n\t"
      "v_add_f32 v216, v216, v212\n\t"
      "v_exp_f32 v215, v39\n\t"
      "v_add_f32 v216, v216, v213\n\t"
      "v_mfma_f32_16x16x32_bf16 v[48:51], a[76:79], v[12:15], v[48:51]\n\t"
      "ds_read_b64_tr_b16 a[104:105], v222 offset:53760\n\t"
      "v_exp_f32 v217, v40\n\t"
      "v_mfma_f32_16x16x32_bf16 v[56:59], a[76:79], v[28:31], v[56:59]\n\t"
      "ds_read_b64_tr_b16 a[106:107], v222 offset:55808\n\t"
      "s_waitcnt lgkmcnt(6)\n\t"
      "v_cvt_pk_bf16_f32 v195, v214, v215\n\t"
      "v_add_f32 v216, v216, v214\n\t"
      "v_mfma_f32_16x16x32_bf16 v[52:55], a[80:83], v[0:3], v[64:67]\n\t"
      "ds_read_b64_tr_b16 a[108:109], v221 offset:57856\n\t"
      "v_exp_f32 v209, v41\n\t"
      "v_mfma_f32_16x16x32_bf16 v[60:63], a[80:83], v[16:19], v[68:71]\n\t"
      "ds_read_b64_tr_b16 a[110:111], v221 offset:59904\n\t"
      "v_add_f32 v216, v216, v215\n\t"
      "v_exp_f32 v210, v42\n\t"
      "v_cvt_pk_bf16_f32 v196, v217, v209\n\t"
      "v_mfma_f32_16x16x32_bf16 a[52:55], a[84:87], v[176:179], a[52:55]\n\t"
      "ds_read_b64_tr_b16 a[112:113], v222 offset:57856\n\t"
      "v_exp_f32 v211, v43\n\t"
      "v_mfma_f32_16x16x32_bf16 a[60:63], a[84:87], v[180:183], a[60:63]\n\t"
      "ds_read_b64_tr_b16 a[114:115], v222 offset:59904\n\t"
      "v_add_f32 v217, v217, v209\n\t"
      "v_exp_f32 v212, v44\n\t"
      "v_mfma_f32_16x16x32_bf16 v[52:55], a[88:91], v[4:7], v[52:55]\n\t"
      "ds_read_b64_tr_b16 a[116:117], v222 offset:12288\n\t"
      "v_cvt_pk_bf16_f32 v197, v210, v211\n\t"
      "v_mfma_f32_16x16x32_bf16 v[60:63], a[88:91], v[20:23], v[60:63]\n\t"
      "ds_read_b64_tr_b16 a[118:119], v222 offset:14336\n\t"
      "s_waitcnt lgkmcnt(8)\n\t"
      "v_add_f32 v217, v217, v210\n\t"
      "v_exp_f32 v213, v45\n\t"
      "v_add_f32 v217, v217, v211\n\t"
      "v_mfma_f32_16x16x32_bf16 v[52:55], a[92:95], v[8:11], v[52:55]\n\t"
      "ds_read_b64_tr_b16 a[120:121], v221 offset:16384\n\t"
      "v_exp_f32 v214, v46\n\t"
      "v_mfma_f32_16x16x32_bf16 v[60:63], a[92:95], v[24:27], v[60:63]\n\t"
      "ds_read_b64_tr_b16 a[122:123], v221 offset:18432\n\t"
      "v_cvt_pk_bf16_f32 v198, v212, v213\n\t"
      "v_add_f32 v217, v217, v212\n\t"
      "v_exp_f32 v215, v47\n\t"
      "v_mfma_f32_16x16x32_bf16 v[52:55], a[96:99], v[12:15], v[52:55]\n\t"
      "ds_read_b64_tr_b16 a[124:125], v222 offset:16384\n\t"
      "v_add_f32 v217, v217, v213\n\t"
      "v_mfma_f32_16x16x32_bf16 v[60:63], a[96:99], v[28:31], v[60:63]\n\t"
      "ds_read_b64_tr_b16 a[126:127], v222 offset:18432\n\t"
      "v_add_f32 v217, v217, v214\n\t"
      "v_cvt_pk_bf16_f32 v199, v214, v215\n\t"
      "v_add_f32 v217, v217, v215\n\t"
      "v_add_f32 v72, v216, v217\n\t"
      "v_mfma_f32_16x16x32_bf16 a[48:51], a[100:103], v[184:187], a[48:51]\n\t"
      "ds_read_b64_tr_b16 a[64:65], v221 offset:20480\n\t"
      "s_waitcnt lgkmcnt(7)\n\t"
      "v_mfma_f32_16x16x32_bf16 a[56:59], a[100:103], v[188:191], a[56:59]\n\t"
      "ds_read_b64_tr_b16 a[66:67], v221 offset:22528\n\t"
      "v_cmp_nge_f32 vcc, %[sLIM], v72\n\t"
      "s_cbranch_vccnz .Lr14_%=\n\t"
      ".Lb14_%=:\n\t"
      "v_add_f32 v218, v218, v216\n\t"
      "v_add_f32 v219, v219, v217\n\t"
      "v_mfma_f32_16x16x32_bf16 a[52:55], a[104:107], v[184:187], a[52:55]\n\t"
      "ds_read_b64_tr_b16 a[68:69], v222 offset:20480\n\t"
      "v_exp_f32 v216, v48\n\t"
      "v_mfma_f32_16x16x32_bf16 a[60:63], a[104:107], v[188:191], a[60:63]\n\t"
      "ds_read_b64_tr_b16 a[70:71], v222 offset:22528\n\t"
      "v_exp_f32 v209, v49\n\t"
      "v_exp_f32 v210, v50\n\t"
      "v_cvt_pk_bf16_f32 v200, v216, v209\n\t"
      "v_mfma_f32_16x16x32_bf16 a[48:51], a[108:111], v[192:195], a[48:51]\n\t"
      "ds_read_b64_tr_b16 a[72:73], v221 offset:24576\n\t"
      "v_exp_f32 v211, v51\n\t"
      "v_mfma_f32_16x16x32_bf16 a[56:59], a[108:111], v[196:199], a[56:59]\n\t"
      "ds_read_b64_tr_b16 a[74:75], v221 offset:26624\n\t"
      "v_add_f32 v216, v216, v209\n\t"
      "v_exp_f32 v212, v52\n\t"
      "v_cvt_pk_bf16_f32 v201, v210, v211\n\t"
      "v_add_f32 v216, v216, v210\n\t"
      "v_mfma_f32_16x16x32_bf16 a[52:55], a[112:115], v[192:195], a[52:55]\n\t"
      "ds_read_b64_tr_b16 a[76:77], v221 offset:61952\n\t"
      "v_exp_f32 v213, v53\n\t"
      "v_mfma_f32_16x16x32_bf16 a[60:63], a[112:115], v[196:199], a[60:63]\n\t"
      "ds_read_b64_tr_b16 a[78:79], v221 offset:64000\n\t"
      "s_waitcnt lgkmcnt(8)\n\t"
      "v_add_f32 v216, v216, v211\n\t"
      "v_exp_f32 v214, v54\n\t"
      "v_cvt_pk_bf16_f32 v202, v212, v213\n\t"
      "v_mfma_f32_16x16x32_bf16 a[36:39], a[116:119], v[104:107], a[36:39]\n\t"
      "ds_read_b64_tr_b16 a[80:81], v222 offset:61952\n\t"
      "v_add_f32 v216, v216, v212\n\t"
      "v_mfma_f32_16x16x32_bf16 a[44:47], a[116:119], v[108:111], a[44:47]\n\t"
      "ds_read_b64_tr_b16 a[82:83], v222 offset:64000\n\t"
      "v_exp_f32 v215, v55\n\t"
      "v_add_f32 v216, v216, v213\n\t"
      "v_exp_f32 v217, v56\n\t"
      "v_cvt_pk_bf16_f32 v203, v214, v215\n\t"
      "v_mfma_f32_16x16x32_bf16 a[32:35], a[120:123], v[112:115], a[32:35]\n\t"
      "ds_read_b64_tr_b16 a[84:85], v222 offset:24576\n\t"
      "v_add_f32 v216, v216, v214\n\t"
      "v_mfma_f32_16x16x32_bf16 a[40:43], a[120:123], v[116:119], a[40:43]\n\t"
      "ds_read_b64_tr_b16 a[86:87], v222 offset:26624\n\t"
      "v_exp_f32 v209, v57\n\t"
      "v_add_f32 v216, v216, v215\n\t"
      "v_exp_f32 v210, v58\n\t"
      "v_mfma_f32_16x16x32_bf16 a[36:39], a[124:127], v[112:115], a[36:39]\n\t"
      "ds_read_b64_tr_b16 a[88:89], v221 offset:28672\n\t"
      "v_cvt_pk_bf16_f32 v204, v217, v209\n\t"
      "v_mfma_f32_16x16x32_bf16 a[44:47], a[124:127], v[116:119], a[44:47]\n\t"
      "ds_read_b64_tr_b16 a[90:91], v221 offset:30720\n\t"
      "s_waitcnt lgkmcnt(8)\n\t"
      "v_exp_f32 v211, v59\n\t"
      "v_add_f32 v217, v217, v209\n\t"
      "v_exp_f32 v212, v60\n\t"
      "v_mfma_f32_16x16x32_bf16 a[32:35], a[64:67], v[120:123], a[32:35]\n\t"
      "ds_read_b64_tr_b16 a[92:93], v222 offset:28672\n\t"
      "v_cvt_pk_bf16_f32 v205, v210, v211\n\t"
      "v_mfma_f32_16x16x32_bf16 a[40:43], a[64:67], v[124:127], a[40:43]\n\t"
      "ds_read_b64_tr_b16 a[94:95], v222 offset:30720\n\t"
      "v_add_f32 v217, v217, v210\n\t"
      "v_exp_f32 v213, v61\n\t"
      "v_add_f32 v217, v217, v211\n\t"
      "v_exp_f32 v214, v62\n\t"
      "v_mfma_f32_16x16x32_bf16 a[36:39], a[68:71], v[120:123], a[36:39]\n\t"
      "ds_read_b64_tr_b16 a[96:97], v221 offset:32768\n\t"
      "v_cvt_pk_bf16_f32 v206, v212, v213\n\t"
      "v_mfma_f32_16x16x32_bf16 a[44:47], a[68:71], v[124:127], a[44:47]\n\t"
      "ds_read_b64_tr_b16 a[98:99], v221 offset:34816\n\t"
      "v_add_f32 v217, v217, v212\n\t"
      "v_exp_f32 v215, v63\n\t"
      "v_add_f32 v217, v217, v213\n\t"
      "v_add_f32 v217, v217, v214\n\t"
      "v_cvt_pk_bf16_f32 v207, v214, v215\n\t"
      "v_mfma_f32_16x16x32_bf16 a[32:35], a[72:75], v[128:131], a[32:35]\n\t"
      "ds_read_b64_tr_b16 a[100:101], v222 offset:32768\n\t"
      "v_add_f32 v217, v217, v215\n\t"
      "v_mfma_f32_16x16x32_bf16 a[40:43], a[72:75], v[132:135], a[40:43]\n\t"
      "ds_read_b64_tr_b16 a[102:103], v222 offset:34816\n\t"
      "s_waitcnt lgkmcnt(8)\n\t"
      "v_add_f32 v72, v216, v217\n\t"
      "v_cmp_nge_f32 vcc, %[sLIM], v72\n\t"
      "s_cbranch_vccnz .Lr15_%=\n\t"
      ".Lb15_%=:\n\t"
      "v_add_f32 v218, v218, v216\n\t"
      "v_add_f32 v219, v219, v217\n\t"
      "v_mfma_f32_16x16x32_bf16 a[48:51], a[76:79], v[200:203], a[48:51]\n\t"
      "ds_read_b64_tr_b16 a[104:105], v221 offset:36864\n\t"
      "v_mov_b32 v228, v218\n\t"
      "v_mfma_f32_16x16x32_bf16 a[56:59], a[76:79], v[204:207], a[56:59]\n\t"
      "ds_read_b64_tr_b16 a[106:107], v221 offset:38912\n\t"
      "v_mov_b32 v229, v219\n\t"
      "v_sub_f32 v252, 0, v64\n\t"
      "v_sub_f32 v254, 0, v68\n\t"
      "v_permlane32_swap_b32 v218, v228\n\t"
      "s_waitcnt vmcnt(4)\n\t"
      "v_cmp_ne_u32 vcc, -1, v234\n\t"
      "s_nop 0\n\t"
      "s_mov_b32 s41, vcc_lo\n\t"
      "v_mfma_f32_16x16x32_bf16 a[52:55], a[80:83], v[200:203], a[52:55]\n\t"
      "ds_read_b64_tr_b16 a[108:109], v222 offset:36864\n\t"
      "v_permlane32_swap_b32 v219, v229\n\t"
      "v_mfma_f32_16x16x32_bf16 a[60:63], a[80:83], v[204:207], a[60:63]\n\t"
      "ds_read_b64_tr_b16 a[110:111], v222 offset:38912\n\t"
      "v_min_u32 v252, -2, v252\n\t"
      "v_add_f32 v218, v218, v228\n\t"
      "v_add_f32 v219, v219, v229\n\t"
      "v_mov_b32 v228, v218\n\t"
      "v_mfma_f32_16x16x32_bf16 a[36:39], a[84:87], v[128:131], a[36:39]\n\t"
      "ds_read_b64_tr_b16 a[112:113], v221 offset:40960\n\t"
      "v_mov_b32 v229, v219\n\t"
      "v_mfma_f32_16x16x32_bf16 a[44:47], a[84:87], v[132:135], a[44:47]\n\t"
      "ds_read_b64_tr_b16 a[114:115], v221 offset:43008\n\t"
      "s_waitcnt lgkmcnt(8)\n\t"
      "v_min_u32 v254, -2, v254\n\t"
      "v_permlane16_swap_b32 v218, v228\n\t"
      "v_permlane16_swap_b32 v219, v229\n\t"
      "s_nop 0\n\t"
      "v_mfma_f32_16x16x32_bf16 a[32:35], a[88:91], v[136:139], a[32:35]\n\t"
      "ds_read_b64_tr_b16 a[116:117], v222 offset:40960\n\t"
      "v_add_f32 v253, v218, v228\n\t"
      "v_mfma_f32_16x16x32_bf16 a[40:43], a[88:91], v[140:143], a[40:43]\n\t"
      "ds_read_b64_tr_b16 a[118:119], v222 offset:43008\n\t"
      "v_add_f32 v255, v219, v229\n\t"
      "v_min_u32 v253, -2, v253\n\t"
      "v_min_u32 v255, -2, v255\n\t"
      "s_mov_b64 exec, 0xffff\n\t"
      "global_store_dwordx4 v238, v[252:255], %[sD3] offset:3584 sc1\n\t"
      "global_store_dwordx4 v238, v[252:255], %[sD2] offset:3328 sc1\n\t"
      "global_store_dwordx4 v238, v[252:255], %[sD1] offset:3072 sc1\n\t"
      "s_mov_b64 exec, -1\n\t"
      "v_mfma_f32_16x16x32_bf16 a[36:39], a[92:95], v[136:139], a[36:39]\n\t"
      "ds_read_b64_tr_b16 a[120:121], v221 offset:45056\n\t"
      "v_accvgpr_read_b32 v56, a48\n\t"
      "v_mfma_f32_16x16x32_bf16 a[44:47], a[92:95], v[140:143], a[44:47]\n\t"
      "ds_read_b64_tr_b16 a[122:123], v221 offset:47104\n\t"
      "v_accvgpr_read_b32 v57, a49\n\t"
      "v_accvgpr_read_b32 v58, a50\n\t"
      "v_accvgpr_read_b32 v59, a51\n\t"
      "v_accvgpr_read_b32 v60, a52\n\t"
      "v_mfma_f32_16x16x32_bf16 a[32:35], a[96:99], v[144:147], a[32:35]\n\t"
      "ds_read_b64_tr_b16 a[124:125], v222 offset:45056\n\t"
      "v_accvgpr_read_b32 v61, a53\n\t"
      "v_mfma_f32_16x16x32_bf16 a[40:43], a[96:99], v[148:151], a[40:43]\n\t"
      "ds_read_b64_tr_b16 a[126:127], v222 offset:47104\n\t"
      "s_waitcnt lgkmcnt(8)\n\t"
      "v_accvgpr_read_b32 v62, a54\n\t"
      "v_accvgpr_read_b32 v63, a55\n\t"
      "v_cvt_pk_bf16_f32 v32, v56, v57\n\t"
      "v_cvt_pk_bf16_f32 v33, v58, v59\n\t"
      "v_mfma_f32_16x16x32_bf16 a[36:39], a[100:103], v[144:147], a[36:39]\n\t"
      "ds_read_b64_tr_b16 a[64:65], v221 offset:49152\n\t"
      "v_cvt_pk_bf16_f32 v34, v60, v61\n\t"
      "v_mfma_f32_16x16x32_bf16 a[44:47], a[100:103], v[148:151], a[44:47]\n\t"
      "ds_read_b64_tr_b16 a[66:67], v221 offset:51200\n\t"
      "v_cvt_pk_bf16_f32 v35, v62, v63\n\t"
      "v_min_u32 v32, -2, v32\n\t"
      "v_min_u32 v33, -2, v33\n\t"
      "v_min_u32 v34, -2, v34\n\t"
      "v_mfma_f32_16x16x32_bf16 a[32:35], a[104:107], v[152:155], a[32:35]\n\t"
      "ds_read_b64_tr_b16 a[68:69], v222 offset:49152\n\t"
      "v_min_u32 v35, -2, v35\n\t"
      "v_mfma_f32_16x16x32_bf16 a[40:43], a[104:107], v[156:159], a[40:43]\n\t"
      "ds_read_b64_tr_b16 a[70:71], v222 offset:51200\n\t"
      "v_mfma_f32_16x16x32_bf16 a[36:39], a[108:111], v[152:155], a[36:39]\n\t"
      "ds_read_b64_tr_b16 a[72:73], v221 offset:53248\n\t"
      "v_accvgpr_read_b32 v56, a56\n\t"
      "v_mfma_f32_16x16x32_bf16 a[44:47], a[108:111], v[156:159], a[44:47]\n\t"
      "ds_read_b64_tr_b16 a[74:75], v221 offset:55296\n\t"
      "global_store_dwordx4 v233, v[32:35], %[sD3] offset:1024 sc1\n\t"
      "s_waitcnt lgkmcnt(8)\n\t"
      "v_accvgpr_read_b32 v57, a57\n\t"
      "v_accvgpr_read_b32 v58, a58\n\t"
      "v_accvgpr_read_b32 v59, a59\n\t"
      "v_accvgpr_read_b32 v60, a60\n\t"
      "v_mfma_f32_16x16x32_bf16 a[32:35], a[112:115], v[160:163], a[32:35]\n\t"
      "ds_read_b64_tr_b16 a[76:77], v222 offset:53248\n\t"
      "v_accvgpr_read_b32 v61, a61\n\t"
      "v_mfma_f32_16x16x32_bf16 a[40:43], a[112:115], v[164:167], a[40:43]\n\t"
      "ds_read_b64_tr_b16 a[78:79], v222 offset:55296\n\t"
      "v_accvgpr_read_b32 v62, a62\n\t"
      "v_accvgpr_read_b32 v63, a63\n\t"
      "v_cvt_pk_bf16_f32 v36, v56, v57\n\t"
      "v_cvt_pk_bf16_f32 v37, v58, v59\n\t"
      "v_mfma_f32_16x16x32_bf16 a[36:39], a[116:119], v[160:163], a[36:39]\n\t"
      "ds_read_b64_tr_b16 a[80:81], v221 offset:57344\n\t"
      "v_cvt_pk_bf16_f32 v38, v60, v61\n\t"
      "v_mfma_f32_16x16x32_bf16 a[44:47], a[116:119], v[164:167], a[44:47]\n\t"
      "ds_read_b64_tr_b16 a[82:83], v221 offset:59392\n\t"
      "v_cvt_pk_bf16_f32 v39, v62, v63\n\t"
      "v_min_u32 v36, -2, v36\n\t"
      "v_min_u32 v37, -2, v37\n\t"
      "v_min_u32 v38, -2, v38\n\t"
      "v_mfma_f32_16x16x32_bf16 a[32:35], a[120:123], v[168:171], a[32:35]\n\t"
      "ds_read_b64_tr_b16 a[84:85], v222 offset:57344\n\t"
      "v_min_u32 v39, -2, v39\n\t"
      "v_mfma_f32_16x16x32_bf16 a[40:43], a[120:123], v[172:175], a[40:43]\n\t"
      "ds_read_b64_tr_b16 a[86:87], v222 offset:59392\n\t"
      "s_waitcnt lgkmcnt(8)\n\t"
      "v_mfma_f32_16x16x32_bf16 a[36:39], a[124:127], v[168:171], a[36:39]\n\t"
      "ds_read_b64_tr_b16 a[88:89], v221 offset:61440\n\t"
      "v_mfma_f32_16x16x32_bf16 a[44:47], a[124:127], v[172:175], a[44:47]\n\t"
      "ds_read_b64_tr_b16 a[90:91], v221 offset:63488\n\t"
      "global_store_dwordx4 v233, v[36:39], %[sD3] offset:2048 sc1\n\t"
      "v_mfma_f32_16x16x32_bf16 a[32:35], a[64:67], v[176:179], a[32:35]\n\t"
      "ds_read_b64_tr_b16 a[92:93], v222 offset:61440\n\t"
      "v_mfma_f32_16x16x32_bf16 a[40:43], a[64:67], v[180:183], a[40:43]\n\t"
      "ds_read_b64_tr_b16 a[94:95], v222 offset:63488\n\t"
      "v_mfma_f32_16x16x32_bf16 a[36:39], a[68:71], v[176:179], a[36:39]\n\t"
      "v_mfma_f32_16x16x32_bf16 a[44:47], a[68:71], v[180:183], a[44:47]\n\t"
      "s_waitcnt vmcnt(5) lgkmcnt(6)\n\t"
      "s_barrier\n\t"
      "ds_read_b64_tr_b16 a[96:97], v223 offset:512\n\t"
      "ds_read_b64_tr_b16 a[98:99], v223 offset:2560\n\t"
      "v_mfma_f32_16x16x32_bf16 a[32:35], a[72:75], v[184:187], a[32:35]\n\t"
      "ds_read_b64_tr_b16 a[100:101], v236 offset:512\n\t"
      "v_mfma_f32_16x16x32_bf16 a[40:43], a[72:75], v[188:191], a[40:43]\n\t"
      "ds_read_b64_tr_b16 a[102:103], v236 offset:2560\n\t"
      "v_mfma_f32_16x16x32_bf16 a[36:39], a[76:79], v[184:187], a[36:39]\n\t"
      "ds_read_b64_tr_b16 a[104:105], v223 offset:4608\n\t"
      "v_mfma_f32_16x16x32_bf16 a[44:47], a[76:79], v[188:191], a[44:47]\n\t"
      "ds_read_b64_tr_b16 a[106:107], v223 offset:6656\n\t"
      "v_mfma_f32_16x16x32_bf16 a[32:35], a[80:83], v[192:195], a[32:35]\n\t"
      "ds_read_b64_tr_b16 a[108:109], v236 offset:4608\n\t"
      "s_waitcnt lgkmcnt(7)\n\t"
      "v_mfma_f32_16x16x32_bf16 a[40:43], a[80:83], v[196:199], a[40:43]\n\t"
      "ds_read_b64_tr_b16 a[110:111], v236 offset:6656\n\t"
      "v_mfma_f32_16x16x32_bf16 a[36:39], a[84:87], v[192:195], a[36:39]\n\t"
      "ds_read_b64_tr_b16 a[112:113], v223 offset:8704\n\t"
      "v_mfma_f32_16x16x32_bf16 a[44:47], a[84:87], v[196:199], a[44:47]\n\t"
      "ds_read_b64_tr_b16 a[114:115], v223 offset:10752\n\t"
      "v_mfma_f32_16x16x32_bf16 a[32:35], a[88:91], v[200:203], a[32:35]\n\t"
      "ds_read_b64_tr_b16 a[116:117], v236 offset:8704\n\t"
      "v_mfma_f32_16x16x32_bf16 a[40:43], a[88:91], v[204:207], a[40:43]\n\t"
      "ds_read_b64_tr_b16 a[118:119], v236 offset:10752\n\t"
      "v_mfma_f32_16x16x32_bf16 a[36:39], a[92:95], v[200:203], a[36:39]\n\t"
      "ds_read_b64_tr_b16 a[120:121], v223 offset:12800\n\t"
      "s_waitcnt lgkmcnt(7)\n\t"
      "v_mfma_f32_16x16x32_bf16 a[44:47], a[92:95], v[204:207], a[44:47]\n\t"
      "ds_read_b64_tr_b16 a[122:123], v223 offset:14848\n\t"
      "v_mfma_f32_16x16x32_bf16 v[16:19], a[96:99], v[80:83], 0\n\t"
      "ds_read_b64_tr_b16 a[124:125], v236 offset:12800\n\t"
      "v_mfma_f32_16x16x32_bf16 v[24:27], a[96:99], v[84:87], 0\n\t"
      "ds_read_b64_tr_b16 a[126:127], v236 offset:14848\n\t"
      "v_mfma_f32_16x16x32_bf16 v[20:23], a[100:103], v[80:83], 0\n\t"
      "ds_read_b64_tr_b16 a[64:65], v223 offset:16896\n\t"
      "v_accvgpr_read_b32 v56, a32\n\t"
      "v_mfma_f32_16x16x32_bf16 v[28:31], a[100:103], v[84:87], 0\n\t"
      "ds_read_b64_tr_b16 a[66:67], v223 offset:18944\n\t"
      "v_accvgpr_read_b32 v57, a33\n\t"
      "v_accvgpr_read_b32 v58, a34\n\t"
      "v_accvgpr_read_b32 v59, a35\n\t"
      "v_accvgpr_read_b32 v60, a36\n\t"
      "v_mfma_f32_16x16x32_bf16 v[16:19], a[104:107], v[88:91], v[16:19]\n\t"
      "ds_read_b64_tr_b16 a[68:69], v236 offset:16896\n\t"
      "v_accvgpr_read_b32 v61, a37\n\t"
      "v_mfma_f32_16x16x32_bf16 v[24:27], a[104:107], v[92:95], v[24:27]\n\t"
      "ds_read_b64_tr_b16 a[70:71], v236 offset:18944\n\t"
      "s_waitcnt lgkmcnt(8)\n\t"
      "v_accvgpr_read_b32 v62, a38\n\t"
      "v_accvgpr_read_b32 v63, a39\n\t"
      "v_cvt_pk_bf16_f32 v40, v56, v57\n\t"
      "v_cvt_pk_bf16_f32 v41, v58, v59\n\t"
      "v_mfma_f32_16x16x32_bf16 v[20:23], a[108:111], v[88:91], v[20:23]\n\t"
      "ds_read_b64_tr_b16 a[72:73], v223 offset:20992\n\t"
      "v_cvt_pk_bf16_f32 v42, v60, v61\n\t"
      "v_mfma_f32_16x16x32_bf16 v[28:31], a[108:111], v[92:95], v[28:31]\n\t"
      "ds_read_b64_tr_b16 a[74:75], v223 offset:23040\n\t"
      "v_cvt_pk_bf16_f32 v43, v62, v63\n\t"
      "v_min_u32 v40, -2, v40\n\t"
      "v_min_u32 v41, -2, v41\n\t"
      "v_min_u32 v42, -2, v42\n\t"
      "v_mfma_f32_16x16x32_bf16 v[16:19], a[112:115], v[96:99], v[16:19]\n\t"
      "ds_read_b64_tr_b16 a[76:77], v236 offset:20992\n\t"
      "v_min_u32 v43, -2, v43\n\t"
      "v_mfma_f32_16x16x32_bf16 v[24:27], a[112:115], v[100:103], v[24:27]\n\t"
      "ds_read_b64_tr_b16 a[78:79], v236 offset:23040\n\t"
      "v_mfma_f32_16x16x32_bf16 v[20:23], a[116:119], v[96:99], v[20:23]\n\t"
      "ds_read_b64_tr_b16 a[80:81], v223 offset:25088\n\t"
      "v_accvgpr_read_b32 v56, a40\n\t"
      "v_mfma_f32_16x16x32_bf16 v[28:31], a[116:119], v[100:103], v[28:31]\n\t"
      "ds_read_b64_tr_b16 a[82:83], v223 offset:27136\n\t"
      "global_store_dwordx4 v233, v[40:43], %[sD2] offset:-1024 sc1\n\t"
      "s_waitcnt lgkmcnt(8)\n\t"
      "v_accvgpr_read_b32 v57, a41\n\t"
      "v_accvgpr_read_b32 v58, a42\n\t"
      "v_accvgpr_read_b32 v59, a43\n\t"
      "v_accvgpr_read_b32 v60, a44\n\t"
      "v_mfma_f32_16x16x32_bf16 v[16:19], a[120:123], v[104:107], v[16:19]\n\t"
      "ds_read_b64_tr_b16 a[84:85], v236 offset:25088\n\t"
      "v_accvgpr_read_b32 v61, a45\n\t"
      "v_mfma_f32_16x16x32_bf16 v[24:27], a[120:123], v[108:111], v[24:27]\n\t"
      "ds_read_b64_tr_b16 a[86:87], v236 offset:27136\n\t"
      "v_accvgpr_read_b32 v62, a46\n\t"
      "v_accvgpr_read_b32 v63, a47\n\t"
      "v_cvt_pk_bf16_f32 v44, v56, v57\n\t"
      "v_cvt_pk_bf16_f32 v45, v58, v59\n\t"
      "v_mfma_f32_16x16x32_bf16 v[20:23], a[124:127], v[104:107], v[20:23]\n\t"
      "ds_read_b64_tr_b16 a[88:89], v223 offset:29184\n\t"
      "v_cvt_pk_bf16_f32 v46, v60, v61\n\t"
      "v_mfma_f32_16x16x32_bf16 v[28:31], a[124:127], v[108:111], v[28:31]\n\t"
      "ds_read_b64_tr_b16 a[90:91], v223 offset:31232\n\t"
      "v_cvt_pk_bf16_f32 v47, v62, v63\n\t"
      "v_min_u32 v44, -2, v44\n\t"
      "v_min_u32 v45, -2, v45\n\t"
      "v_min_u32 v46, -2, v46\n\t"
      "v_mfma_f32_16x16x32_bf16 v[16:19], a[64:67], v[112:115], v[16:19]\n\t"
      "ds_read_b64_tr_b16 a[92:93], v236 offset:29184\n\t"
      "v_min_u32 v47, -2, v47\n\t"
      "v_mfma_f32_16x16x32_bf16 v[24:27], a[64:67], v[116:119], v[24:27]\n\t"
      "ds_read_b64_tr_b16 a[94:95], v236 offset:31232\n\t"
      "s_waitcnt lgkmcnt(8)\n\t"
      "v_mfma_f32_16x16x32_bf16 v[20:23], a[68:71], v[112:115], v[20:23]\n\t"
      "ds_read_b64_tr_b16 a[96:97], v223 offset:33280\n\t"
      "v_mfma_f32_16x16x32_bf16 v[28:31], a[68:71], v[116:119], v[28:31]\n\t"
      "ds_read_b64_tr_b16 a[98:99], v223 offset:35328\n\t"
      "global_store_dwordx4 v233, v[44:47], %[sD2] offset:0 sc1\n\t"
      "v_mfma_f32_16x16x32_bf16 v[16:19], a[72:75], v[120:123], v[16:19]\n\t"
      "ds_read_b64_tr_b16 a[100:101], v236 offset:33280\n\t"
      "v_mfma_f32_16x16x32_bf16 v[24:27], a[72:75], v[124:127], v[24:27]\n\t"
      "ds_read_b64_tr_b16 a[102:103], v236 offset:35328\n\t"
      "v_mfma_f32_16x16x32_bf16 v[20:23], a[76:79], v[120:123], v[20:23]\n\t"
      "ds_read_b64_tr_b16 a[104:105], v223 offset:37376\n\t"
      "s_waitcnt lgkmcnt(7)\n\t"
      "v_mfma_f32_16x16x32_bf16 v[28:31], a[76:79], v[124:127], v[28:31]\n\t"
      "ds_read_b64_tr_b16 a[106:107], v223 offset:39424\n\t"
      "v_mfma_f32_16x16x32_bf16 v[16:19], a[80:83], v[128:131], v[16:19]\n\t"
      "ds_read_b64_tr_b16 a[108:109], v236 offset:37376\n\t"
      "v_mfma_f32_16x16x32_bf16 v[24:27], a[80:83], v[132:135], v[24:27]\n\t"
      "ds_read_b64_tr_b16 a[110:111], v236 offset:39424\n\t"
      "v_mfma_f32_16x16x32_bf16 v[20:23], a[84:87], v[128:131], v[20:23]\n\t"
      "ds_read_b64_tr_b16 a[112:113], v223 offset:41472\n\t"
      "v_mfma_f32_16x16x32_bf16 v[28:31], a[84:87], v[132:135], v[28:31]\n\t"
      "ds_read_b64_tr_b16 a[114:115], v223 offset:43520\n\t"
      "v_mfma_f32_16x16x32_bf16 v[16:19], a[88:91], v[136:139], v[16:19]\n\t"
      "ds_read_b64_tr_b16 a[116:117], v236 offset:41472\n\t"
      "s_waitcnt lgkmcnt(7)\n\t"
      "v_mfma_f32_16x16x32_bf16 v[24:27], a[88:91], v[140:143], v[24:27]\n\t"
      "ds_read_b64_tr_b16 a[118:119], v236 offset:43520\n\t"
      "v_mfma_f32_16x16x32_bf16 v[20:23], a[92:95], v[136:139], v[20:23]\n\t"
      "ds_read_b64_tr_b16 a[120:121], v223 offset:45568\n\t"
      "v_mfma_f32_16x16x32_bf16 v[28:31], a[92:95], v[140:143], v[28:31]\n\t"
      "ds_read_b64_tr_b16 a[122:123], v223 offset:47616\n\t"
      "v_mfma_f32_16x16x32_bf16 v[16:19], a[96:99], v[144:147], v[16:19]\n\t"
      "ds_read_b64_tr_b16 a[124:125], v236 offset:45568\n\t"
      "v_mfma_f32_16x16x32_bf16 v[24:27], a[96:99], v[148:151], v[24:27]\n\t"
      "ds_read_b64_tr_b16 a[126:127], v236 offset:47616\n\t"
      "v_mfma_f32_16x16x32_bf16 v[20:23], a[100:103], v[144:147], v[20:23]\n\t"
      "ds_read_b64_tr_b16 a[64:65], v223 offset:49664\n\t"
      "s_waitcnt lgkmcnt(7)\n\t"
      "v_mfma_f32_16x16x32_bf16 v[28:31], a[100:103], v[148:151], v[28:31]\n\t"
      "ds_read_b64_tr_b16 a[66:67], v223 offset:51712\n\t"
      "v_mfma_f32_16x16x32_bf16 v[16:19], a[104:107], v[152:155], v[16:19]\n\t"
      "ds_read_b64_tr_b16 a[68:69], v236 offset:49664\n\t"
      "v_mfma_f32_16x16x32_bf16 v[24:27], a[104:107], v[156:159], v[24:27]\n\t"
      "ds_read_b64_tr_b16 a[70:71], v236 offset:51712\n\t"
      "v_mfma_f32_16x16x32_bf16 v[20:23], a[108:111], v[152:155], v[20:23]\n\t"
      "ds_read_b64_tr_b16 a[72:73], v223 offset:53760\n\t"
      "v_mfma_f32_16x16x32_bf16 v[28:31], a[108:111], v[156:159], v[28:31]\n\t"
      "ds_read_b64_tr_b16 a[74:75], v223 offset:55808\n\t"
      "v_mfma_f32_16x16x32_bf16 v[16:19], a[112:115], v[160:163], v[16:19]\n\t"
      "ds_read_b64_tr_b16 a[76:77], v236 offset:53760\n\t"
      "s_waitcnt lgkmcnt(7)\n\t"
      "v_mfma_f32_16x16x32_bf16 v[24:27], a[112:115], v[164:167], v[24:27]\n\t"
      "ds_read_b64_tr_b16 a[78:79], v236 offset:55808\n\t"
      "v_mfma_f32_16x16x32_bf16 v[20:23], a[116:119], v[160:163], v[20:23]\n\t"
      "ds_read_b64_tr_b16 a[80:81], v223 offset:57856\n\t"
      "v_mfma_f32_16x16x32_bf16 v[28:31], a[116:119], v[164:167], v[28:31]\n\t"
      "ds_read_b64_tr_b16 a[82:83], v223 offset:59904\n\t"
      "v_mfma_f32_16x16x32_bf16 v[16:19], a[120:123], v[168:171], v[16:19]\n\t"
      "ds_read_b64_tr_b16 a[84:85], v236 offset:57856\n\t"
      "v_mfma_f32_16x16x32_bf16 v[24:27], a[120:123], v[172:175], v[24:27]\n\t"
      "ds_read_b64_tr_b16 a[86:87], v236 offset:59904\n\t"
      "v_mfma_f32_16x16x32_bf16 v[20:23], a[124:127], v[168:171], v[20:23]\n\t"
      "ds_read_b64_tr_b16 a[88:89], v223 offset:61952\n\t"
      "s_waitcnt lgkmcnt(7)\n\t"
      "v_mfma_f32_16x16x32_bf16 v[28:31], a[124:127], v[172:175], v[28:31]\n\t"
      "ds_read_b64_tr_b16 a[90:91], v223 offset:64000\n\t"
      "v_mfma_f32_16x16x32_bf16 v[16:19], a[64:67], v[176:179], v[16:19]\n\t"
      "ds_read_b64_tr_b16 a[92:93], v236 offset:61952\n\t"
      "v_mfma_f32_16x16x32_bf16 v[24:27], a[64:67], v[180:183], v[24:27]\n\t"
      "ds_read_b64_tr_b16 a[94:95], v236 offset:64000\n\t"
      "v_mfma_f32_16x16x32_bf16 v[20:23], a[68:71], v[176:179], v[20:23]\n\t"
      "ds_read_b64_tr_b16 a[96:97], v223 offset:0\n\t"
      "v_mfma_f32_16x16x32_bf16 v[28:31], a[68:71], v[180:183], v[28:31]\n\t"
      "ds_read_b64_tr_b16 a[98:99], v223 offset:2048\n\t"
      "global_load_dwordx4 v[64:67], v233, %[sIN] offset:1024 sc1\n\t"
      "global_load_dwordx4 v[68:71], v233, %[sIN] offset:2048 sc1\n\t"
      "v_mfma_f32_16x16x32_bf16 v[16:19], a[72:75], v[184:187], v[16:19]\n\t"
      "ds_read_b64_tr_b16 a[100:101], v236 offset:0\n\t"
      "s_waitcnt lgkmcnt(7)\n\t"
      "v_mfma_f32_16x16x32_bf16 v[24:27], a[72:75], v[188:191], v[24:27]\n\t"
      "ds_read_b64_tr_b16 a[102:103], v236 offset:2048\n\t"
      "global_load_dwordx4 v[248:251], v238, %[sIN] offset:3584 sc1\n\t"
      "global_load_dwordx4 v[212:215], v238, %[sIN] offset:3328 sc1\n\t"
      "v_mfma_f32_16x16x32_bf16 v[20:23], a[76:79], v[184:187], v[20:23]\n\t"
      "ds_read_b64_tr_b16 a[104:105], v223 offset:4096\n\t"
      "v_mfma_f32_16x16x32_bf16 v[28:31], a[76:79], v[188:191], v[28:31]\n\t"
      "ds_read_b64_tr_b16 a[106:107], v223 offset:6144\n\t"
      "global_load_dwordx4 v[208:211], v238, %[sIN] offset:3072 sc1\n\t"
      "v_mfma_f32_16x16x32_bf16 v[16:19], a[80:83], v[192:195], v[16:19]\n\t"
      "ds_read_b64_tr_b16 a[108:109], v236 offset:4096\n\t"
      "v_mfma_f32_16x16x32_bf16 v[24:27], a[80:83], v[196:199], v[24:27]\n\t"
      "ds_read_b64_tr_b16 a[110:111], v236 offset:6144\n\t"
      "v_mfma_f32_16x16x32_bf16 v[20:23], a[84:87], v[192:195], v[20:23]\n\t"
      "ds_read_b64_tr_b16 a[112:113], v223 offset:8192\n\t"
      "s_waitcnt lgkmcnt(7)\n\t"
      "v_mfma_f32_16x16x32_bf16 v[28:31], a[84:87], v[196:199], v[28:31]\n\t"
      "ds_read_b64_tr_b16 a[114:115], v223 offset:10240\n\t"
      "v_mfma_f32_16x16x32_bf16 v[16:19], a[88:91], v[200:203], v[16:19]\n\t"
      "ds_read_b64_tr_b16 a[116:117], v236 offset:8192\n\t"
      "v_mfma_f32_16x16x32_bf16 v[24:27], a[88:91], v[204:207], v[24:27]\n\t"
      "ds_read_b64_tr_b16 a[118:119], v236 offset:10240\n\t"
      "v_mfma_f32_16x16x32_bf16 v[20:23], a[92:95], v[200:203], v[20:23]\n\t"
      "ds_read_b64_tr_b16 a[120:121], v223 offset:12288\n\t"
      "v_mfma_f32_16x16x32_bf16 v[28:31], a[92:95], v[204:207], v[28:31]\n\t"
      "ds_read_b64_tr_b16 a[122:123], v223 offset:14336\n\t"
      "v_mfma_f32_16x16x32_bf16 v[0:3], a[96:99], v[80:83], 0\n\t"
      "ds_read_b64_tr_b16 a[124:125], v236 offset:12288\n\t"
      "s_waitcnt lgkmcnt(7)\n\t"
      "v_mfma_f32_16x16x32_bf16 v[8:11], a[96:99], v[84:87], 0\n\t"
      "ds_read_b64_tr_b16 a[126:127], v236 offset:14336\n\t"
      "v_mfma_f32_16x16x32_bf16 v[4:7], a[100:103], v[80:83], 0\n\t"
      "ds_read_b64_tr_b16 a[64:65], v223 offset:16384\n\t"
      "v_cvt_pk_bf16_f32 v48, v16, v17\n\t"
      "v_mfma_f32_16x16x32_bf16 v[12:15], a[100:103], v[84:87], 0\n\t"
      "ds_read_b64_tr_b16 a[66:67], v223 offset:18432\n\t"
      "v_cvt_pk_bf16_f32 v49, v18, v19\n\t"
      "v_cvt_pk_bf16_f32 v50, v20, v21\n\t"
      "v_cvt_pk_bf16_f32 v51, v22, v23\n\t"
      "v_min_u32 v48, -2, v48\n\t"
      "v_mfma_f32_16x16x32_bf16 v[0:3], a[104:107], v[88:91], v[0:3]\n\t"
      "ds_read_b64_tr_b16 a[68:69], v236 offset:16384\n\t"
      "v_min_u32 v49, -2, v49\n\t"
      "v_mfma_f32_16x16x32_bf16 v[8:11], a[104:107], v[92:95], v[8:11]\n\t"
      "ds_read_b64_tr_b16 a[70:71], v236 offset:18432\n\t"
      "v_min_u32 v50, -2, v50\n\t"
      "v_min_u32 v51, -2, v51\n\t"
      "v_mfma_f32_16x16x32_bf16 v[4:7], a[108:111], v[88:91], v[4:7]\n\t"
      "ds_read_b64_tr_b16 a[72:73], v223 offset:20480\n\t"
      "v_cvt_pk_bf16_f32 v52, v24, v25\n\t"
      "v_mfma_f32_16x16x32_bf16 v[12:15], a[108:111], v[92:95], v[12:15]\n\t"
      "ds_read_b64_tr_b16 a[74:75], v223 offset:22528\n\t"
      "global_store_dwordx4 v233, v[48:51], %[sD1] offset:-3072 sc1\n\t"
      "s_waitcnt lgkmcnt(8)\n\t"
      "v_cvt_pk_bf16_f32 v53, v26, v27\n\t"
      "v_cvt_pk_bf16_f32 v54, v28, v29\n\t"
      "v_cvt_pk_bf16_f32 v55, v30, v31\n\t"
      "v_min_u32 v52, -2, v52\n\t"
      "v_mfma_f32_16x16x32_bf16 v[0:3], a[112:115], v[96:99], v[0:3]\n\t"
      "ds_read_b64_tr_b16 a[76:77], v236 offset:20480\n\t"
      "v_min_u32 v53, -2, v53\n\t"
      "v_mfma_f32_16x16x32_bf16 v[8:11], a[112:115], v[100:103], v[8:11]\n\t"
      "ds_read_b64_tr_b16 a[78:79], v236 offset:22528\n\t"
      "v_min_u32 v54, -2, v54\n\t"
      "v_min_u32 v55, -2, v55\n\t"
      "v_mfma_f32_16x16x32_bf16 v[4:7], a[116:119], v[96:99], v[4:7]\n\t"
      "ds_read_b64_tr_b16 a[80:81], v223 offset:24576\n\t"
      "v_mfma_f32_16x16x32_bf16 v[12:15], a[116:119], v[100:103], v[12:15]\n\t"
      "ds_read_b64_tr_b16 a[82:83], v223 offset:26624\n\t"
      "global_store_dwordx4 v233, v[52:55], %[sD1] offset:-2048 sc1\n\t"
      "v_mfma_f32_16x16x32_bf16 v[0:3], a[120:123], v[104:107], v[0:3]\n\t"
      "ds_read_b64_tr_b16 a[84:85], v236 offset:24576\n\t"
      "s_waitcnt lgkmcnt(7)\n\t"
      "v_mfma_f32_16x16x32_bf16 v[8:11], a[120:123], v[108:111], v[8:11]\n\t"
      "ds_read_b64_tr_b16 a[86:87], v236 offset:26624\n\t"
      "v_mfma_f32_16x16x32_bf16 v[4:7], a[124:127], v[104:107], v[4:7]\n\t"
      "ds_read_b64_tr_b16 a[88:89], v223 offset:28672\n\t"
      "v_mfma_f32_16x16x32_bf16 v[12:15], a[124:127], v[108:111], v[12:15]\n\t"
      "ds_read_b64_tr_b16 a[90:91], v223 offset:30720\n\t"
      "v_mfma_f32_16x16x32_bf16 v[0:3], a[64:67], v[112:115], v[0:3]\n\t"
      "ds_read_b64_tr_b16 a[92:93], v236 offset:28672\n\t"
      "v_mfma_f32_16x16x32_bf16 v[8:11], a[64:67], v[116:119], v[8:11]\n\t"
      "ds_read_b64_tr_b16 a[94:95], v236 offset:30720\n\t"
      "v_mfma_f32_16x16x32_bf16 v[4:7], a[68:71], v[112:115], v[4:7]\n\t"
      "ds_read_b64_tr_b16 a[96:97], v223 offset:32768\n\t"
      "s_waitcnt lgkmcnt(7)\n\t"
      "v_mfma_f32_16x16x32_bf16 v[12:15], a[68:71], v[116:119], v[12:15]\n\t"
      "ds_read_b64_tr_b16 a[98:99], v223 offset:34816\n\t"
      "v_mfma_f32_16x16x32_bf16 v[0:3], a[72:75], v[120:123], v[0:3]\n\t"
      "ds_read_b64_tr_b16 a[100:101], v236 offset:32768\n\t"
      "v_mfma_f32_16x16x32_bf16 v[8:11], a[72:75], v[124:127], v[8:11]\n\t"
      "ds_read_b64_tr_b16 a[102:103], v236 offset:34816\n\t"
      "v_mfma_f32_16x16x32_bf16 v[4:7], a[76:79], v[120:123], v[4:7]\n\t"
      "ds_read_b64_tr_b16 a[104:105], v223 offset:36864\n\t"
      "v_mfma_f32_16x16x32_bf16 v[12:15], a[76:79], v[124:127], v[12:15]\n\t"
      "ds_read_b64_tr_b16 a[106:107], v223 offset:38912\n\t"
      "v_mfma_f32_16x16x32_bf16 v[0:3], a[80:83], v[128:131], v[0:3]\n\t"
      "ds_read_b64_tr_b16 a[108:109], v236 offset:36864\n\t"
      "s_waitcnt lgkmcnt(7)\n\t"
      "v_mfma_f32_16x16x32_bf16 v[8:11], a[80:83], v[132:135], v[8:11]\n\t"
      "ds_read_b64_tr_b16 a[110:111], v236 offset:38912\n\t"
      "v_mfma_f32_16x16x32_bf16 v[4:7], a[84:87], v[128:131], v[4:7]\n\t"
      "ds_read_b64_tr_b16 a[112:113], v223 offset:40960\n\t"
      "v_mfma_f32_16x16x32_bf16 v[12:15], a[84:87], v[132:135], v[12:15]\n\t"
      "ds_read_b64_tr_b16 a[114:115], v223 offset:43008\n\t"
      "v_mfma_f32_16x16x32_bf16 v[0:3], a[88:91], v[136:139], v[0:3]\n\t"
      "ds_read_b64_tr_b16 a[116:117], v236 offset:40960\n\t"
      "v_mfma_f32_16x16x32_bf16 v[8:11], a[88:91], v[140:143], v[8:11]\n\t"
      "ds_read_b64_tr_b16 a[118:119], v236 offset:43008\n\t"
      "v_mfma_f32_16x16x32_bf16 v[4:7], a[92:95], v[136:139], v[4:7]\n\t"
      "ds_read_b64_tr_b16 a[120:121], v223 offset:45056\n\t"
      "s_waitcnt lgkmcnt(7)\n\t"
      "v_mfma_f32_16x16x32_bf16 v[12:15], a[92:95], v[140:143], v[12:15]\n\t"
      "ds_read_b64_tr_b16 a[122:123], v223 offset:47104\n\t"
      "v_mfma_f32_16x16x32_bf16 v[0:3], a[96:99], v[144:147], v[0:3]\n\t"
      "ds_read_b64_tr_b16 a[124:125], v236 offset:45056\n\t"
      "v_mfma_f32_16x16x32_bf16 v[8:11], a[96:99], v[148:151], v[8:11]\n\t"
      "ds_read_b64_tr_b16 a[126:127], v236 offset:47104\n\t"
      "v_mfma_f32_16x16x32_bf16 v[4:7], a[100:103], v[144:147], v[4:7]\n\t"
      "ds_read_b64_tr_b16 a[64:65], v223 offset:49152\n\t"
      "v_mfma_f32_16x16x32_bf16 v[12:15], a[100:103], v[148:151], v[12:15]\n\t"
      "ds_read_b64_tr_b16 a[66:67], v223 offset:51200\n\t"
      "v_mfma_f32_16x16x32_bf16 v[0:3], a[104:107], v[152:155], v[0:3]\n\t"
      "ds_read_b64_tr_b16 a[68:69], v236 offset:49152\n\t"
      "s_waitcnt lgkmcnt(7)\n\t"
      "v_mfma_f32_16x16x32_bf16 v[8:11], a[104:107], v[156:159], v[8:11]\n\t"
      "ds_read_b64_tr_b16 a[70:71], v236 offset:51200\n\t"
      "v_mfma_f32_16x16x32_bf16 v[4:7], a[108:111], v[152:155], v[4:7]\n\t"
      "ds_read_b64_tr_b16 a[72:73], v223 offset:53248\n\t"
      "v_mfma_f32_16x16x32_bf16 v[12:15], a[108:111], v[156:159], v[12:15]\n\t"
      "ds_read_b64_tr_b16 a[74:75], v223 offset:55296\n\t"
      "v_mfma_f32_16x16x32_bf16 v[0:3], a[112:115], v[160:163], v[0:3]\n\t"
      "ds_read_b64_tr_b16 a[76:77], v236 offset:53248\n\t"
      "v_mfma_f32_16x16x32_bf16 v[8:11], a[112:115], v[164:167], v[8:11]\n\t"
      "ds_read_b64_tr_b16 a[78:79], v236 offset:55296\n\t"
      "v_mfma_f32_16x16x32_bf16 v[4:7], a[116:119], v[160:163], v[4:7]\n\t"
      "ds_read_b64_tr_b16 a[80:81], v223 offset:57344\n\t"
      "s_waitcnt lgkmcnt(7)\n\t"
      "v_mfma_f32_16x16x32_bf16 v[12:15], a[116:119], v[164:167], v[12:15]\n\t"
      "ds_read_b64_tr_b16 a[82:83], v223 offset:59392\n\t"
      "v_mfma_f32_16x16x32_bf16 v[0:3], a[120:123], v[168:171], v[0:3]\n\t"
      "ds_read_b64_tr_b16 a[84:85], v236 offset:57344\n\t"
      "v_mfma_f32_16x16x32_bf16 v[8:11], a[120:123], v[172:175], v[8:11]\n\t"
      "ds_read_b64_tr_b16 a[86:87], v236 offset:59392\n\t"
      "v_mfma_f32_16x16x32_bf16 v[4:7], a[124:127], v[168:171], v[4:7]\n\t"
      "ds_read_b64_tr_b16 a[88:89], v223 offset:61440\n\t"
      "v_mfma_f32_16x16x32_bf16 v[12:15], a[124:127], v[172:175], v[12:15]\n\t"
      "ds_read_b64_tr_b16 a[90:91], v223 offset:63488\n\t"
      "v_mfma_f32_16x16x32_bf16 v[0:3], a[64:67], v[176:179], v[0:3]\n\t"
      "ds_read_b64_tr_b16 a[92:93], v236 offset:61440\n\t"
      "s_waitcnt lgkmcnt(7)\n\t"
      "v_mfma_f32_16x16x32_bf16 v[8:11], a[64:67], v[180:183], v[8:11]\n\t"
      "ds_read_b64_tr_b16 a[94:95], v236 offset:63488\n\t"
      "v_mfma_f32_16x16x32_bf16 v[4:7], a[68:71], v[176:179], v[4:7]\n\t"
      "v_mfma_f32_16x16x32_bf16 v[12:15], a[68:71], v[180:183], v[12:15]\n\t"
      "v_mfma_f32_16x16x32_bf16 v[0:3], a[72:75], v[184:187], v[0:3]\n\t"
      "v_mfma_f32_16x16x32_bf16 v[8:11], a[72:75], v[188:191], v[8:11]\n\t"
      "v_mfma_f32_16x16x32_bf16 v[4:7], a[76:79], v[184:187], v[4:7]\n\t"
      "s_waitcnt lgkmcnt(2)\n\t"
      "v_mfma_f32_16x16x32_bf16 v[12:15], a[76:79], v[188:191], v[12:15]\n\t"
      "global_load_dwordx4 v[72:75], v233, %[sIN] offset:-1024 sc1\n\t"
      "global_load_dwordx4 v[76:79], v233, %[sIN] offset:0 sc1\n\t"
      "v_mfma_f32_16x16x32_bf16 v[0:3], a[80:83], v[192:195], v[0:3]\n\t"
      "v_mfma_f32_16x16x32_bf16 v[8:11], a[80:83], v[196:199], v[8:11]\n\t"
      "v_mfma_f32_16x16x32_bf16 v[4:7], a[84:87], v[192:195], v[4:7]\n\t"
      "v_mfma_f32_16x16x32_bf16 v[12:15], a[84:87], v[196:199], v[12:15]\n\t"
      "v_mfma_f32_16x16x32_bf16 v[0:3], a[88:91], v[200:203], v[0:3]\n\t"
      "s_waitcnt lgkmcnt(0)\n\t"
      "v_mfma_f32_16x16x32_bf16 v[8:11], a[88:91], v[204:207], v[8:11]\n\t"
      "v_mfma_f32_16x16x32_bf16 v[4:7], a[92:95], v[200:203], v[4:7]\n\t"
      "v_mfma_f32_16x16x32_bf16 v[12:15], a[92:95], v[204:207], v[12:15]\n\t"
      "s_waitcnt vmcnt(4)\n\t"
      "s_nop 8\n\t"
      ".subsection 1\n\t"
      ".Lr0_%=:\n\t"
      "s_getpc_b64 s[58:59]\n\t"
      "s_branch .Lrare0_%=\n\t"
      "v_mov_b32 v80, v248\n\t"
      "v_mov_b32 v81, v249\n\t"
      "v_mov_b32 v82, v250\n\t"
      "v_mov_b32 v83, v251\n\t"
      "v_mov_b32 v84, v252\n\t"
      "v_mov_b32 v85, v253\n\t"
      "v_mov_b32 v86, v254\n\t"
      "v_mov_b32 v87, v255\n\t"
      "s_nop 1\n\t"
      "s_branch .Lb0_%=\n\t"
      ".Lr1_%=:\n\t"
      "s_getpc_b64 s[58:59]\n\t"
      "s_branch .Lrare1_%=\n\t"
      "v_mov_b32 v88, v248\n\t"
      "v_mov_b32 v89, v249\n\t"
      "v_mov_b32 v90, v250\n\t"
      "v_mov_b32 v91, v251\n\t"
      "v_mov_b32 v92, v252\n\t"
      "v_mov_b32 v93, v253\n\t"
      "v_mov_b32 v94, v254\n\t"
      "v_mov_b32 v95, v255\n\t"
      "s_nop 1\n\t"
      "s_branch .Lb1_%=\n\t"
      ".Lr2_%=:\n\t"
      "s_getpc_b64 s[58:59]\n\t"
      "s_branch .Lrare0_%=\n\t"
      "v_mov_b32 v96, v248\n\t"
      "v_mov_b32 v97, v249\n\t"
      "v_mov_b32 v98, v250\n\t"
      "v_mov_b32 v99, v251\n\t"
      "v_mov_b32 v100, v252\n\t"
      "v_mov_b32 v101, v253\n\t"
      "v_mov_b32 v102, v254\n\t"
      "v_mov_b32 v103, v255\n\t"
      "s_nop 1\n\t"
      "s_branch .Lb2_%=\n\t"
      ".Lr3_%=:\n\t"
      "s_getpc_b64 s[58:59]\n\t"
      "s_branch .Lrare1_%=\n\t"
      "v_mov_b32 v104, v248\n\t"
      "v_mov_b32 v105, v249\n\t"
      "v_mov_b32 v106, v250\n\t"
      "v_mov_b32 v107, v251\n\t"
      "v_mov_b32 v108, v252\n\t"
      "v_mov_b32 v109, v253\n\t"
      "v_mov_b32 v110, v254\n\t"
      "v_mov_b32 v111, v255\n\t"
      "s_nop 1\n\t"
      "s_branch .Lb3_%=\n\t"
      ".Lr4_%=:\n\t"
      "s_getpc_b64 s[58:59]\n\t"
      "s_branch .Lrare0_%=\n\t"
      "v_mov_b32 v112, v248\n\t"
      "v_mov_b32 v113, v249\n\t"
      "v_mov_b32 v114, v250\n\t"
      "v_mov_b32 v115, v251\n\t"
      "v_mov_b32 v116, v252\n\t"
      "v_mov_b32 v117, v253\n\t"
      "v_mov_b32 v118, v254\n\t"
      "v_mov_b32 v119, v255\n\t"
      "s_nop 1\n\t"
      "s_branch .Lb4_%=\n\t"
      ".Lr5_%=:\n\t"
      "s_getpc_b64 s[58:59]\n\t"
      "s_branch .Lrare1_%=\n\t"
      "v_mov_b32 v120, v248\n\t"
      "v_mov_b32 v121, v249\n\t"
      "v_mov_b32 v122, v250\n\t"
      "v_mov_b32 v123, v251\n\t"
      "v_mov_b32 v124, v252\n\t"
      "v_mov_b32 v125, v253\n\t"
      "v_mov_b32 v126, v254\n\t"
      "v_mov_b32 v127, v255\n\t"
      "s_nop 1\n\t"
      "s_branch .Lb5_%=\n\t"
      ".Lr6_%=:\n\t"
      "s_getpc_b64 s[58:59]\n\t"
      "s_branch .Lrare0_%=\n\t"
      "v_mov_b32 v128, v248\n\t"
      "v_mov_b32 v129, v249\n\t"
      "v_mov_b32 v130, v250\n\t"
      "v_mov_b32 v131, v251\n\t"
      "v_mov_b32 v132, v252\n\t"
      "v_mov_b32 v133, v253\n\t"
      "v_mov_b32 v134, v254\n\t"
      "v_mov_b32 v135, v255\n\t"
      "s_nop 1\n\t"
      "s_branch .Lb6_%=\n\t"
      ".Lr7_%=:\n\t"
      "s_getpc_b64 s[58:59]\n\t"
      "s_branch .Lrare1_%=\n\t"
      "v_mov_b32 v136, v248\n\t"
      "v_mov_b32 v137, v249\n\t"
      "v_mov_b32 v138, v250\n\t"
      "v_mov_b32 v139, v251\n\t"
      "v_mov_b32 v140, v252\n\t"
      "v_mov_b32 v141, v253\n\t"
      "v_mov_b32 v142, v254\n\t"
      "v_mov_b32 v143, v255\n\t"
      "s_nop 1\n\t"
      "s_branch .Lb7_%=\n\t"
      ".Lr8_%=:\n\t"
      "s_getpc_b64 s[58:59]\n\t"
      "s_branch .Lrare0_%=\n\t"
      "v_mov_b32 v144, v248\n\t"
      "v_mov_b32 v145, v249\n\t"
      "v_mov_b32 v146, v250\n\t"
      "v_mov_b32 v147, v251\n\t"
      "v_mov_b32 v148, v252\n\t"
      "v_mov_b32 v149, v253\n\t"
      "v_mov_b32 v150, v254\n\t"
      "v_mov_b32 v151, v255\n\t"
      "s_nop 1\n\t"
      "s_branch .Lb8_%=\n\t"
      ".Lr9_%=:\n\t"
      "s_getpc_b64 s[58:59]\n\t"
      "s_branch .Lrare1_%=\n\t"
      "v_mov_b32 v152, v248\n\t"
      "v_mov_b32 v153, v249\n\t"
      "v_mov_b32 v154, v250\n\t"
      "v_mov_b32 v155, v251\n\t"
      "v_mov_b32 v156, v252\n\t"
      "v_mov_b32 v157, v253\n\t"
      "v_mov_b32 v158, v254\n\t"
      "v_mov_b32 v159, v255\n\t"
      "s_nop 1\n\t"
      "s_branch .Lb9_%=\n\t"
      ".Lr10_%=:\n\t"
      "s_getpc_b64 s[58:59]\n\t"
      "s_branch .Lrare0_%=\n\t"
      "v_mov_b32 v160, v248\n\t"
      "v_mov_b32 v161, v249\n\t"
      "v_mov_b32 v162, v250\n\t"
      "v_mov_b32 v163, v251\n\t"
      "v_mov_b32 v164, v252\n\t"
      "v_mov_b32 v165, v253\n\t"
      "v_mov_b32 v166, v254\n\t"
      "v_mov_b32 v167, v255\n\t"
      "s_nop 1\n\t"
      "s_branch .Lb10_%=\n\t"
      ".Lr11_%=:\n\t"
      "s_getpc_b64 s[58:59]\n\t"
      "s_branch .Lrare1_%=\n\t"
      "v_mov_b32 v168, v248\n\t"
      "v_mov_b32 v169, v249\n\t"
      "v_mov_b32 v170, v250\n\t"
      "v_mov_b32 v171, v251\n\t"
      "v_mov_b32 v172, v252\n\t"
      "v_mov_b32 v173, v253\n\t"
      "v_mov_b32 v174, v254\n\t"
      "v_mov_b32 v175, v255\n\t"
      "s_nop 1\n\t"
      "s_branch .Lb11_%=\n\t"
      ".Lr12_%=:\n\t"
      "s_getpc_b64 s[58:59]\n\t"
      "s_branch .Lrare0_%=\n\t"
      "v_mov_b32 v176, v248\n\t"
      "v_mov_b32 v177, v249\n\t"
      "v_mov_b32 v178, v250\n\t"
      "v_mov_b32 v179, v251\n\t"
      "v_mov_b32 v180, v252\n\t"
      "v_mov_b32 v181, v253\n\t"
      "v_mov_b32 v182, v254\n\t"
      "v_mov_b32 v183, v255\n\t"
      "s_nop 1\n\t"
      "s_branch .Lb12_%=\n\t"
      ".Lr13_%=:\n\t"
      "s_getpc_b64 s[58:59]\n\t"
      "s_branch .Lrare1_%=\n\t"
      "v_mov_b32 v184, v248\n\t"
      "v_mov_b32 v185, v249\n\t"
      "v_mov_b32 v186, v250\n\t"
      "v_mov_b32 v187, v251\n\t"
      "v_mov_b32 v188, v252\n\t"
      "v_mov_b32 v189, v253\n\t"
      "v_mov_b32 v190, v254\n\t"
      "v_mov_b32 v191, v255\n\t"
      "s_nop 1\n\t"
      "s_branch .Lb13_%=\n\t"
      ".Lr14_%=:\n\t"
      "s_getpc_b64 s[58:59]\n\t"
      "s_branch .Lrare0_%=\n\t"
      "v_mov_b32 v192, v248\n\t"
      "v_mov_b32 v193, v249\n\t"
      "v_mov_b32 v194, v250\n\t"
      "v_mov_b32 v195, v251\n\t"
      "v_mov_b32 v196, v252\n\t"
      "v_mov_b32 v197, v253\n\t"
      "v_mov_b32 v198, v254\n\t"
      "v_mov_b32 v199, v255\n\t"
      "s_nop 1\n\t"
      "s_branch .Lb14_%=\n\t"
      ".Lr15_%=:\n\t"
      "s_getpc_b64 s[58:59]\n\t"
      "s_branch .Lrare1_%=\n\t"
      "v_mov_b32 v200, v248\n\t"
      "v_mov_b32 v201, v249\n\t"
      "v_mov_b32 v202, v250\n\t"
      "v_mov_b32 v203, v251\n\t"
      "v_mov_b32 v204, v252\n\t"
      "v_mov_b32 v205, v253\n\t"
      "v_mov_b32 v206, v254\n\t"
      "v_mov_b32 v207, v255\n\t"
      "s_nop 1\n\t"
      "s_branch .Lb15_%=\n\t"
      ".Lrare0_%=:\n\t"
      "s_nop 15\n\t"
      "v_max3_f32 v228, v32, v33, v34\n\t"
      "v_max3_f32 v229, v40, v41, v42\n\t"
      "v_max3_f32 v228, v228, v35, v36\n\t"
      "v_max3_f32 v229, v229, v43, v44\n\t"
      "v_max3_f32 v228, v228, v37, v38\n\t"
      "v_max3_f32 v229, v229, v45, v46\n\t"
      "v_max_f32 v228, v228, v39\n\t"
      "v_max_f32 v229, v229, v47\n\t"
      "v_mov_b32 v75, v228\n\t"
      "v_mov_b32 v76, v229\n\t"
      "s_nop 0\n\t"
      "v_permlane32_swap_b32 v228, v75\n\t"
      "v_permlane32_swap_b32 v229, v76\n\t"
      "s_nop 0\n\t"
      "v_max_f32 v228, v228, v75\n\t"
      "v_max_f32 v229, v229, v76\n\t"
      "v_mov_b32 v75, v228\n\t"
      "v_mov_b32 v76, v229\n\t"
      "s_nop 0\n\t"
      "v_permlane16_swap_b32 v228, v75\n\t"
      "v_permlane16_swap_b32 v229, v76\n\t"
      "s_nop 0\n\t"
      "v_max_f32 v228, v228, v75\n\t"
      "v_max_f32 v229, v229, v76\n\t"
      "v_ceil_f32 v228, v228\n\t"
      "v_ceil_f32 v229, v229\n\t"
      "v_max_f32 v228, 0, v228\n\t"
      "v_exp_f32 v73, -v228\n\t"
      "v_max_f32 v229, 0, v229\n\t"
      "v_exp_f32 v74, -v229\n\t"
      "v_sub_f32 v32, v32, v228\n\t"
      "v_sub_f32 v48, v48, v228\n\t"
      "v_sub_f32 v33, v33, v228\n\t"
      "v_sub_f32 v49, v49, v228\n\t"
      "v_sub_f32 v34, v34, v228\n\t"
      "v_sub_f32 v50, v50, v228\n\t"
      "v_sub_f32 v35, v35, v228\n\t"
      "v_sub_f32 v51, v51, v228\n\t"
      "v_sub_f32 v36, v36, v228\n\t"
      "v_sub_f32 v52, v52, v228\n\t"
      "v_sub_f32 v37, v37, v228\n\t"
      "v_sub_f32 v53, v53, v228\n\t"
      "v_sub_f32 v38, v38, v228\n\t"
      "v_sub_f32 v54, v54, v228\n\t"
      "v_sub_f32 v39, v39, v228\n\t"
      "v_sub_f32 v55, v55, v228\n\t"
      "v_sub_f32 v40, v40, v229\n\t"
      "v_sub_f32 v56, v56, v229\n\t"
      "v_sub_f32 v41, v41, v229\n\t"
      "v_sub_f32 v57, v57, v229\n\t"
      "v_sub_f32 v42, v42, v229\n\t"
      "v_sub_f32 v58, v58, v229\n\t"
      "v_sub_f32 v43, v43, v229\n\t"
      "v_sub_f32 v59, v59, v229\n\t"
      "v_sub_f32 v44, v44, v229\n\t"
      "v_sub_f32 v60, v60, v229\n\t"
      "v_sub_f32 v45, v45, v229\n\t"
      "v_sub_f32 v61, v61, v229\n\t"
      "v_sub_f32 v46, v46, v229\n\t"
      "v_sub_f32 v62, v62, v229\n\t"
      "v_sub_f32 v47, v47, v229\n\t"
      "v_sub_f32 v63, v63, v229\n\t"
      "v_sub_f32 v64, v64, v228\n\t"
      "v_sub_f32 v65, v65, v228\n\t"
      "v_sub_f32 v66, v66, v228\n\t"
      "v_sub_f32 v67, v67, v228\n\t"
      "v_sub_f32 v68, v68, v229\n\t"
      "v_sub_f32 v69, v69, v229\n\t"
      "v_sub_f32 v70, v70, v229\n\t"
      "v_sub_f32 v71, v71, v229\n\t"
      "v_exp_f32 v216, v32\n\t"
      "v_exp_f32 v209, v33\n\t"
      "v_exp_f32 v210, v34\n\t"
      "v_cvt_pk_bf16_f32 v248, v216, v209\n\t"
      "v_exp_f32 v211, v35\n\t"
      "v_add_f32 v216, v216, v209\n\t"
      "v_exp_f32 v212, v36\n\t"
      "v_cvt_pk_bf16_f32 v249, v210, v211\n\t"
      "v_add_f32 v216, v216, v210\n\t"
      "v_exp_f32 v213, v37\n\t"
      "v_add_f32 v216, v216, v211\n\t"
      "v_exp_f32 v214, v38\n\t"
      "v_cvt_pk_bf16_f32 v250, v212, v213\n\t"
      "v_add_f32 v216, v216, v212\n\t"
      "v_exp_f32 v215, v39\n\t"
      "v_add_f32 v216, v216, v213\n\t"
      "v_exp_f32 v217, v40\n\t"
      "v_cvt_pk_bf16_f32 v251, v214, v215\n\t"
      "v_add_f32 v216, v216, v214\n\t"
      "v_exp_f32 v209, v41\n\t"
      "v_add_f32 v216, v216, v215\n\t"
      "v_exp_f32 v210, v42\n\t"
      "v_cvt_pk_bf16_f32 v252, v217, v209\n\t"
      "v_exp_f32 v211, v43\n\t"
      "v_add_f32 v217, v217, v209\n\t"
      "v_exp_f32 v212, v44\n\t"
      "v_cvt_pk_bf16_f32 v253, v210, v211\n\t"
      "v_add_f32 v217, v217, v210\n\t"
      "v_exp_f32 v213, v45\n\t"
      "v_add_f32 v217, v217, v211\n\t"
      "v_exp_f32 v214, v46\n\t"
      "v_cvt_pk_bf16_f32 v254, v212, v213\n\t"
      "v_add_f32 v217, v217, v212\n\t"
      "v_exp_f32 v215, v47\n\t"
      "v_add_f32 v217, v217, v213\n\t"
      "v_add_f32 v217, v217, v214\n\t"
      "v_cvt_pk_bf16_f32 v255, v214, v215\n\t"
      "v_add_f32 v217, v217, v215\n\t"
      "v_add_f32 v72, v216, v217\n\t"
      "s_branch .Lrarec_%=\n\t"
      ".Lrare1_%=:\n\t"
      "s_nop 15\n\t"
      "v_max3_f32 v228, v48, v49, v50\n\t"
      "v_max3_f32 v229, v56, v57, v58\n\t"
      "v_max3_f32 v228, v228, v51, v52\n\t"
      "v_max3_f32 v229, v229, v59, v60\n\t"
      "v_max3_f32 v228, v228, v53, v54\n\t"
      "v_max3_f32 v229, v229, v61, v62\n\t"
      "v_max_f32 v228, v228, v55\n\t"
      "v_max_f32 v229, v229, v63\n\t"
      "v_mov_b32 v75, v228\n\t"
      "v_mov_b32 v76, v229\n\t"
      "s_nop 0\n\t"
      "v_permlane32_swap_b32 v228, v75\n\t"
      "v_permlane32_swap_b32 v229, v76\n\t"
      "s_nop 0\n\t"
      "v_max_f32 v228, v228, v75\n\t"
      "v_max_f32 v229, v229, v76\n\t"
      "v_mov_b32 v75, v228\n\t"
      "v_mov_b32 v76, v229\n\t"
      "s_nop 0\n\t"
      "v_permlane16_swap_b32 v228, v75\n\t"
      "v_permlane16_swap_b32 v229, v76\n\t"
      "s_nop 0\n\t"
      "v_max_f32 v228, v228, v75\n\t"
      "v_max_f32 v229, v229, v76\n\t"
      "v_ceil_f32 v228, v228\n\t"
      "v_ceil_f32 v229, v229\n\t"
      "v_max_f32 v228, 0, v228\n\t"
      "v_exp_f32 v73, -v228\n\t"
      "v_max_f32 v229, 0, v229\n\t"
      "v_exp_f32 v74, -v229\n\t"
      "v_sub_f32 v48, v48, v228\n\t"
      "v_sub_f32 v32, v32, v228\n\t"
      "v_sub_f32 v49, v49, v228\n\t"
      "v_sub_f32 v33, v33, v228\n\t"
      "v_sub_f32 v50, v50, v228\n\t"
      "v_sub_f32 v34, v34, v228\n\t"
      "v_sub_f32 v51, v51, v228\n\t"
      "v_sub_f32 v35, v35, v228\n\t"
      "v_sub_f32 v52, v52, v228\n\t"
      "v_sub_f32 v36, v36, v228\n\t"
      "v_sub_f32 v53, v53, v228\n\t"
      "v_sub_f32 v37, v37, v228\n\t"
      "v_sub_f32 v54, v54, v228\n\t"
      "v_sub_f32 v38, v38, v228\n\t"
      "v_sub_f32 v55, v55, v228\n\t"
      "v_sub_f32 v39, v39, v228\n\t"
      "v_sub_f32 v56, v56, v229\n\t"
      "v_sub_f32 v40, v40, v229\n\t"
      "v_sub_f32 v57, v57, v229\n\t"
      "v_sub_f32 v41, v41, v229\n\t"
      "v_sub_f32 v58, v58, v229\n\t"
      "v_sub_f32 v42, v42, v229\n\t"
      "v_sub_f32 v59, v59, v229\n\t"
      "v_sub_f32 v43, v43, v229\n\t"
      "v_sub_f32 v60, v60, v229\n\t"
      "v_sub_f32 v44, v44, v229\n\t"
      "v_sub_f32 v61, v61, v229\n\t"
      "v_sub_f32 v45, v45, v229\n\t"
      "v_sub_f32 v62, v62, v229\n\t"
      "v_sub_f32 v46, v46, v229\n\t"
      "v_sub_f32 v63, v63, v229\n\t"
      "v_sub_f32 v47, v47, v229\n\t"
      "v_sub_f32 v64, v64, v228\n\t"
      "v_sub_f32 v65, v65, v228\n\t"
      "v_sub_f32 v66, v66, v228\n\t"
      "v_sub_f32 v67, v67, v228\n\t"
      "v_sub_f32 v68, v68, v229\n\t"
      "v_sub_f32 v69, v69, v229\n\t"
      "v_sub_f32 v70, v70, v229\n\t"
      "v_sub_f32 v71, v71, v229\n\t"
      "v_exp_f32 v216, v48\n\t"
      "v_exp_f32 v209, v49\n\t"
      "v_exp_f32 v210, v50\n\t"
      "v_cvt_pk_bf16_f32 v248, v216, v209\n\t"
      "v_exp_f32 v211, v51\n\t"
      "v_add_f32 v216, v216, v209\n\t"
      "v_exp_f32 v212, v52\n\t"
      "v_cvt_pk_bf16_f32 v249, v210, v211\n\t"
      "v_add_f32 v216, v216, v210\n\t"
      "v_exp_f32 v213, v53\n\t"
      "v_add_f32 v216, v216, v211\n\t"
      "v_exp_f32 v214, v54\n\t"
      "v_cvt_pk_bf16_f32 v250, v212, v213\n\t"
      "v_add_f32 v216, v216, v212\n\t"
      "v_exp_f32 v215, v55\n\t"
      "v_add_f32 v216, v216, v213\n\t"
      "v_exp_f32 v217, v56\n\t"
      "v_cvt_pk_bf16_f32 v251, v214, v215\n\t"
      "v_add_f32 v216, v216, v214\n\t"
      "v_exp_f32 v209, v57\n\t"
      "v_add_f32 v216, v216, v215\n\t"
      "v_exp_f32 v210, v58\n\t"
      "v_cvt_pk_bf16_f32 v252, v217, v209\n\t"
      "v_exp_f32 v211, v59\n\t"
      "v_add_f32 v217, v217, v209\n\t"
      "v_exp_f32 v212, v60\n\t"
      "v_cvt_pk_bf16_f32 v253, v210, v211\n\t"
      "v_add_f32 v217, v217, v210\n\t"
      "v_exp_f32 v213, v61\n\t"
      "v_add_f32 v217, v217, v211\n\t"
      "v_exp_f32 v214, v62\n\t"
      "v_cvt_pk_bf16_f32 v254, v212, v213\n\t"
      "v_add_f32 v217, v217, v212\n\t"
      "v_exp_f32 v215, v63\n\t"
      "v_add_f32 v217, v217, v213\n\t"
      "v_add_f32 v217, v217, v214\n\t"
      "v_cvt_pk_bf16_f32 v255, v214, v215\n\t"
      "v_add_f32 v217, v217, v215\n\t"
      "v_add_f32 v72, v216, v217\n\t"
      ".Lrarec_%=:\n\t"
      "v_accvgpr_read_b32 v208, a48\n\t"
      "v_mul_f32 v208, v208, v73\n\t"
      "v_accvgpr_write_b32 a48, v208\n\t"
      "v_accvgpr_read_b32 v209, a49\n\t"
      "v_mul_f32 v209, v209, v73\n\t"
      "v_accvgpr_write_b32 a49, v209\n\t"
      "v_accvgpr_read_b32 v210, a50\n\t"
      "v_mul_f32 v210, v210, v73\n\t"
      "v_accvgpr_write_b32 a50, v210\n\t"
      "v_accvgpr_read_b32 v211, a51\n\t"
      "v_mul_f32 v211, v211, v73\n\t"
      "v_accvgpr_write_b32 a51, v211\n\t"
      "v_accvgpr_read_b32 v212, a52\n\t"
      "v_mul_f32 v212, v212, v73\n\t"
      "v_accvgpr_write_b32 a52, v212\n\t"
      "v_accvgpr_read_b32 v213, a53\n\t"
      "v_mul_f32 v213, v213, v73\n\t"
      "v_accvgpr_write_b32 a53, v213\n\t"
      "v_accvgpr_read_b32 v214, a54\n\t"
      "v_mul_f32 v214, v214, v73\n\t"
      "v_accvgpr_write_b32 a54, v214\n\t"
      "v_accvgpr_read_b32 v215, a55\n\t"
      "v_mul_f32 v215, v215, v73\n\t"
      "v_accvgpr_write_b32 a55, v215\n\t"
      "v_accvgpr_read_b32 v208, a56\n\t"
      "v_mul_f32 v208, v208, v74\n\t"
      "v_accvgpr_write_b32 a56, v208\n\t"
      "v_accvgpr_read_b32 v209, a57\n\t"
      "v_mul_f32 v209, v209, v74\n\t"
      "v_accvgpr_write_b32 a57, v209\n\t"
      "v_accvgpr_read_b32 v210, a58\n\t"
      "v_mul_f32 v210, v210, v74\n\t"
      "v_accvgpr_write_b32 a58, v210\n\t"
      "v_accvgpr_read_b32 v211, a59\n\t"
      "v_mul_f32 v211, v211, v74\n\t"
      "v_accvgpr_write_b32 a59, v211\n\t"
      "v_accvgpr_read_b32 v212, a60\n\t"
      "v_mul_f32 v212, v212, v74\n\t"
      "v_accvgpr_write_b32 a60, v212\n\t"
      "v_accvgpr_read_b32 v213, a61\n\t"
      "v_mul_f32 v213, v213, v74\n\t"
      "v_accvgpr_write_b32 a61, v213\n\t"
      "v_accvgpr_read_b32 v214, a62\n\t"
      "v_mul_f32 v214, v214, v74\n\t"
      "v_accvgpr_write_b32 a62, v214\n\t"
      "v_accvgpr_read_b32 v215, a63\n\t"
      "v_mul_f32 v215, v215, v74\n\t"
      "v_accvgpr_write_b32 a63, v215\n\t"
      "v_accvgpr_read_b32 v208, a32\n\t"
      "v_mul_f32 v208, v208, v73\n\t"
      "v_accvgpr_write_b32 a32, v208\n\t"
      "v_accvgpr_read_b32 v209, a33\n\t"
      "v_mul_f32 v209, v209, v73\n\t"
      "v_accvgpr_write_b32 a33, v209\n\t"
      "v_accvgpr_read_b32 v210, a34\n\t"
      "v_mul_f32 v210, v210, v73\n\t"
      "v_accvgpr_write_b32 a34, v210\n\t"
      "v_accvgpr_read_b32 v211, a35\n\t"
      "v_mul_f32 v211, v211, v73\n\t"
      "v_accvgpr_write_b32 a35, v211\n\t"
      "v_accvgpr_read_b32 v212, a36\n\t"
      "v_mul_f32 v212, v212, v73\n\t"
      "v_accvgpr_write_b32 a36, v212\n\t"
      "v_accvgpr_read_b32 v213, a37\n\t"
      "v_mul_f32 v213, v213, v73\n\t"
      "v_accvgpr_write_b32 a37, v213\n\t"
      "v_accvgpr_read_b32 v214, a38\n\t"
      "v_mul_f32 v214, v214, v73\n\t"
      "v_accvgpr_write_b32 a38, v214\n\t"
      "v_accvgpr_read_b32 v215, a39\n\t"
      "v_mul_f32 v215, v215, v73\n\t"
      "v_accvgpr_write_b32 a39, v215\n\t"
      "v_accvgpr_read_b32 v208, a40\n\t"
      "v_mul_f32 v208, v208, v74\n\t"
      "v_accvgpr_write_b32 a40, v208\n\t"
      "v_accvgpr_read_b32 v209, a41\n\t"
      "v_mul_f32 v209, v209, v74\n\t"
      "v_accvgpr_write_b32 a41, v209\n\t"
      "v_accvgpr_read_b32 v210, a42\n\t"
      "v_mul_f32 v210, v210, v74\n\t"
      "v_accvgpr_write_b32 a42, v210\n\t"
      "v_accvgpr_read_b32 v211, a43\n\t"
      "v_mul_f32 v211, v211, v74\n\t"
      "v_accvgpr_write_b32 a43, v211\n\t"
      "v_accvgpr_read_b32 v212, a44\n\t"
      "v_mul_f32 v212, v212, v74\n\t"
      "v_accvgpr_write_b32 a44, v212\n\t"
      "v_accvgpr_read_b32 v213, a45\n\t"
      "v_mul_f32 v213, v213, v74\n\t"
      "v_accvgpr_write_b32 a45, v213\n\t"
      "v_accvgpr_read_b32 v214, a46\n\t"
      "v_mul_f32 v214, v214, v74\n\t"
      "v_accvgpr_write_b32 a46, v214\n\t"
      "v_accvgpr_read_b32 v215, a47\n\t"
      "v_mul_f32 v215, v215, v74\n\t"
      "v_accvgpr_write_b32 a47, v215\n\t"
      "v_mul_f32 v218, v218, v73\n\t"
      "v_mul_f32 v219, v219, v74\n\t"
      "v_min_f32 v228, 0x437f0000, v228\n\t"
      "v_cvt_u32_f32 v228, v228\n\t"
      "v_lshlrev_b32 v228, 7, v228\n\t"
      "v_lshl_or_b32 v228, v228, 16, v228\n\t"
      "v_min_f32 v229, 0x437f0000, v229\n\t"
      "v_cvt_u32_f32 v229, v229\n\t"
      "v_lshlrev_b32 v229, 7, v229\n\t"
      "v_lshl_or_b32 v229, v229, 16, v229\n\t"
      "v_pk_sub_u16 v80, v80, v228 clamp\n\t"
      "v_pk_sub_u16 v81, v81, v228 clamp\n\t"
      "v_pk_sub_u16 v82, v82, v228 clamp\n\t"
      "v_pk_sub_u16 v83, v83, v228 clamp\n\t"
      "v_pk_sub_u16 v84, v84, v229 clamp\n\t"
      "v_pk_sub_u16 v85, v85, v229 clamp\n\t"
      "v_pk_sub_u16 v86, v86, v229 clamp\n\t"
      "v_pk_sub_u16 v87, v87, v229 clamp\n\t"
      "v_pk_sub_u16 v88, v88, v228 clamp\n\t"
      "v_pk_sub_u16 v89, v89, v228 clamp\n\t"
      "v_pk_sub_u16 v90, v90, v228 clamp\n\t"
      "v_pk_sub_u16 v91, v91, v228 clamp\n\t"
      "v_pk_sub_u16 v92, v92, v229 clamp\n\t"
      "v_pk_sub_u16 v93, v93, v229 clamp\n\t"
      "v_pk_sub_u16 v94, v94, v229 clamp\n\t"
      "v_pk_sub_u16 v95, v95, v229 clamp\n\t"
      "v_pk_sub_u16 v96, v96, v228 clamp\n\t"
      "v_pk_sub_u16 v97, v97, v228 clamp\n\t"
      "v_pk_sub_u16 v98, v98, v228 clamp\n\t"
      "v_pk_sub_u16 v99, v99, v228 clamp\n\t"
      "v_pk_sub_u16 v100, v100, v229 clamp\n\t"
      "v_pk_sub_u16 v101, v101, v229 clamp\n\t"
      "v_pk_sub_u16 v102, v102, v229 clamp\n\t"
      "v_pk_sub_u16 v103, v103, v229 clamp\n\t"
      "v_pk_sub_u16 v104, v104, v228 clamp\n\t"
      "v_pk_sub_u16 v105, v105, v228 clamp\n\t"
      "v_pk_sub_u16 v106, v106, v228 clamp\n\t"
      "v_pk_sub_u16 v107, v107, v228 clamp\n\t"
      "v_pk_sub_u16 v108, v108, v229 clamp\n\t"
      "v_pk_sub_u16 v109, v109, v229 clamp\n\t"
      "v_pk_sub_u16 v110, v110, v229 clamp\n\t"
      "v_pk_sub_u16 v111, v111, v229 clamp\n\t"
      "v_pk_sub_u16 v112, v112, v228 clamp\n\t"
      "v_pk_sub_u16 v113, v113, v228 clamp\n\t"
      "v_pk_sub_u16 v114, v114, v228 clamp\n\t"
      "v_pk_sub_u16 v115, v115, v228 clamp\n\t"
      "v_pk_sub_u16 v116, v116, v229 clamp\n\t"
      "v_pk_sub_u16 v117, v117, v229 clamp\n\t"
      "v_pk_sub_u16 v118, v118, v229 clamp\n\t"
      "v_pk_sub_u16 v119, v119, v229 clamp\n\t"
      "v_pk_sub_u16 v120, v120, v228 clamp\n\t"
      "v_pk_sub_u16 v121, v121, v228 clamp\n\t"
      "v_pk_sub_u16 v122, v122, v228 clamp\n\t"
      "v_pk_sub_u16 v123, v123, v228 clamp\n\t"
      "v_pk_sub_u16 v124, v124, v229 clamp\n\t"
      "v_pk_sub_u16 v125, v125, v229 clamp\n\t"
      "v_pk_sub_u16 v126, v126, v229 clamp\n\t"
      "v_pk_sub_u16 v127, v127, v229 clamp\n\t"
      "v_pk_sub_u16 v128, v128, v228 clamp\n\t"
      "v_pk_sub_u16 v129, v129, v228 clamp\n\t"
      "v_pk_sub_u16 v130, v130, v228 clamp\n\t"
      "v_pk_sub_u16 v131, v131, v228 clamp\n\t"
      "v_pk_sub_u16 v132, v132, v229 clamp\n\t"
      "v_pk_sub_u16 v133, v133, v229 clamp\n\t"
      "v_pk_sub_u16 v134, v134, v229 clamp\n\t"
      "v_pk_sub_u16 v135, v135, v229 clamp\n\t"
      "v_pk_sub_u16 v136, v136, v228 clamp\n\t"
      "v_pk_sub_u16 v137, v137, v228 clamp\n\t"
      "v_pk_sub_u16 v138, v138, v228 clamp\n\t"
      "v_pk_sub_u16 v139, v139, v228 clamp\n\t"
      "v_pk_sub_u16 v140, v140, v229 clamp\n\t"
      "v_pk_sub_u16 v141, v141, v229 clamp\n\t"
      "v_pk_sub_u16 v142, v142, v229 clamp\n\t"
      "v_pk_sub_u16 v143, v143, v229 clamp\n\t"
      "v_pk_sub_u16 v144, v144, v228 clamp\n\t"
      "v_pk_sub_u16 v145, v145, v228 clamp\n\t"
      "v_pk_sub_u16 v146, v146, v228 clamp\n\t"
      "v_pk_sub_u16 v147, v147, v228 clamp\n\t"
      "v_pk_sub_u16 v148, v148, v229 clamp\n\t"
      "v_pk_sub_u16 v149, v149, v229 clamp\n\t"
      "v_pk_sub_u16 v150, v150, v229 clamp\n\t"
      "v_pk_sub_u16 v151, v151, v229 clamp\n\t"
      "v_pk_sub_u16 v152, v152, v228 clamp\n\t"
      "v_pk_sub_u16 v153, v153, v228 clamp\n\t"
      "v_pk_sub_u16 v154, v154, v228 clamp\n\t"
      "v_pk_sub_u16 v155, v155, v228 clamp\n\t"
      "v_pk_sub_u16 v156, v156, v229 clamp\n\t"
      "v_pk_sub_u16 v157, v157, v229 clamp\n\t"
      "v_pk_sub_u16 v158, v158, v229 clamp\n\t"
      "v_pk_sub_u16 v159, v159, v229 clamp\n\t"
      "v_pk_sub_u16 v160, v160, v228 clamp\n\t"
      "v_pk_sub_u16 v161, v161, v228 clamp\n\t"
      "v_pk_sub_u16 v162, v162, v228 clamp\n\t"
      "v_pk_sub_u16 v163, v163, v228 clamp\n\t"
      "v_pk_sub_u16 v164, v164, v229 clamp\n\t"
      "v_pk_sub_u16 v165, v165, v229 clamp\n\t"
      "v_pk_sub_u16 v166, v166, v229 clamp\n\t"
      "v_pk_sub_u16 v167, v167, v229 clamp\n\t"
      "v_pk_sub_u16 v168, v168, v228 clamp\n\t"
      "v_pk_sub_u16 v169, v169, v228 clamp\n\t"
      "v_pk_sub_u16 v170, v170, v228 clamp\n\t"
      "v_pk_sub_u16 v171, v171, v228 clamp\n\t"
      "v_pk_sub_u16 v172, v172, v229 clamp\n\t"
      "v_pk_sub_u16 v173, v173, v229 clamp\n\t"
      "v_pk_sub_u16 v174, v174, v229 clamp\n\t"
      "v_pk_sub_u16 v175, v175, v229 clamp\n\t"
      "v_pk_sub_u16 v176, v176, v228 clamp\n\t"
      "v_pk_sub_u16 v177, v177, v228 clamp\n\t"
      "v_pk_sub_u16 v178, v178, v228 clamp\n\t"
      "v_pk_sub_u16 v179, v179, v228 clamp\n\t"
      "v_pk_sub_u16 v180, v180, v229 clamp\n\t"
      "v_pk_sub_u16 v181, v181, v229 clamp\n\t"
      "v_pk_sub_u16 v182, v182, v229 clamp\n\t"
      "v_pk_sub_u16 v183, v183, v229 clamp\n\t"
      "v_pk_sub_u16 v184, v184, v228 clamp\n\t"
      "v_pk_sub_u16 v185, v185, v228 clamp\n\t"
      "v_pk_sub_u16 v186, v186, v228 clamp\n\t"
      "v_pk_sub_u16 v187, v187, v228 clamp\n\t"
      "v_pk_sub_u16 v188, v188, v229 clamp\n\t"
      "v_pk_sub_u16 v189, v189, v229 clamp\n\t"
      "v_pk_sub_u16 v190, v190, v229 clamp\n\t"
      "v_pk_sub_u16 v191, v191, v229 clamp\n\t"
      "v_pk_sub_u16 v192, v192, v228 clamp\n\t"
      "v_pk_sub_u16 v193, v193, v228 clamp\n\t"
      "v_pk_sub_u16 v194, v194, v228 clamp\n\t"
      "v_pk_sub_u16 v195, v195, v228 clamp\n\t"
      "v_pk_sub_u16 v196, v196, v229 clamp\n\t"
      "v_pk_sub_u16 v197, v197, v229 clamp\n\t"
      "v_pk_sub_u16 v198, v198, v229 clamp\n\t"
      "v_pk_sub_u16 v199, v199, v229 clamp\n\t"
      "s_add_u32 s58, s58, 4\n\t"
      "s_addc_u32 s59, s59, 0\n\t"
      "s_setpc_b64 s[58:59]\n\t"
      ".subsection 0\n\t"
      : "={v[0:15]}"(o0), "={v[252:255]}"(ml), "={v[72:75]}"(m2a), "={v[76:79]}"(m2b), "={v[64:67]}"(m3a), "={v[68:71]}"(m3b),
        "={v[208:211]}"(s1), "={v[212:215]}"(s2), "={v[248:251]}"(s3), "={v[48:51]}"(x1a), "={v[52:55]}"(x1b), "={v[40:43]}"(x2a), "={v[44:47]}"(x2b), "={v[32:35]}"(x3a), "={v[36:39]}"(x3b),
        "=&{s41}"(rdy), "+{v228}"(qra), "+{v245}"(j0), "+{v220}"(ka)
      : "{v221}"(va), "{v222}"(va ^ 32u), "{v223}"(va + 0x10000u), "{v236}"((va ^ 32u) + 0x10000u), "{v224}"(qa), "{v225}"(qc), "{v226}"(qb), "{v227}"(qd), "{v239}"(v0), "{v235}"(v0 + 1024u), "{v246}"(v0 + 2048u), "{v247}"(v0 + 3072u),
        "{v231}"(waddr), "{v232}"(faddr), "{v233}"(lane * 16u), "{v238}"(c16 * 16u),
        [sK] "s"(sKs), [sVH] "s"(sVH), [sVL] "s"(sVL), [sMW] "s"(w * 4096u), [sV01] "s"(w == 1u ? 16u * 8192u : 0u),
        [sFL] "s"(pay), [sCSC] "s"(CSC), [sLIM] "s"(PS_LIMIT), [sIN] "s"(inb), [sPC] "s"(pc), "{v237}"(pfo),
        [sD1] "s"(dp1), [sD2] "s"(dp2), [sD3] "s"(dp3)
      : "v16", "v17", "v18", "v19", "v20", "v21", "v22", "v23", "v24", "v25", "v26", "v27", "v28", "v29", "v30", "v31", "v56", "v57", "v58", "v59", "v60", "v61", "v62", "v63", "v80", "v81", "v82", "v83", "v84", "v85", "v86", "v87", "v88", "v89", "v90", "v91", "v92", "v93", "v94", "v95", "v96", "v97", "v98", "v99", "v100", "v101", "v102", "v103", "v104", "v105", "v106", "v107", "v108", "v109", "v110", "v111", "v112", "v113", "v114", "v115", "v116", "v117", "v118", "v119", "v120", "v121", "v122", "v123", "v124", "v125", "v126", "v127", "v128", "v129", "v130", "v131", "v132", "v133", "v134", "v135", "v136", "v137", "v138", "v139", "v140", "v141", "v142", "v143", "v144", "v145", "v146", "v147", "v148", "v149", "v150", "v151", "v152", "v153", "v154", "v155", "v156", "v157", "v158", "v159", "v160", "v161", "v162", "v163", "v164", "v165", "v166", "v167", "v168", "v169", "v170", "v171", "v172", "v173", "v174", "v175", "v176", "v177", "v178", "v179", "v180", "v181", "v182", "v183", "v184", "v185", "v186", "v187", "v188", "v189", "v190", "v191", "v192", "v193", "v194", "v195", "v196", "v197", "v198", "v199", "v200", "v201", "v202", "v203", "v204", "v205", "v206", "v207", "v216", "v217", "v218", "v219", "v229", "v230", "v234", "a0", "a1", "a2", "a3", "a4", "a5", "a6", "a7", "a8", "a9", "a10", "a11", "a12", "a13", "a14", "a15", "a16", "a17", "a18", "a19", "a20", "a21", "a22", "a23", "a24", "a25", "a26", "a27", "a28", "a29", "a30", "a31", "a32", "a33", "a34", "a35", "a36", "a37", "a38", "a39", "a40", "a41", "a42", "a43", "a44", "a45", "a46", "a47", "a48", "a49", "a50", "a51", "a52", "a53", "a54", "a55", "a56", "a57", "a58", "a59", "a60", "a61", "a62", "a63", "a64", "a65", "a66", "a67", "a68", "a69", "a70", "a71", "a72", "a73", "a74", "a75", "a76", "a77", "a78", "a79", "a80", "a81", "a82", "a83", "a84", "a85", "a86", "a87", "a88", "a89", "a90", "a91", "a92", "a93", "a94", "a95", "a96", "a97", "a98", "a99", "a100", "a101", "a102", "a103", "a104", "a105", "a106", "a107", "a108", "a109", "a110", "a111", "a112", "a113", "a114", "a115", "a116", "a117", "a118", "a119", "a120", "a121", "a122", "a123", "a124", "a125", "a126", "a127", "s40", "s58", "s59", "vcc", "scc", "memory");
#define MAX4(x) max(max(x[0], x[1]), max(x[2], x[3]))
#define EMPTY(v) (__builtin_amdgcn_ballot_w64((v) == 0xffffffffu) != 0ull)
#define LDMSG(c, ma, mb, sv) asm volatile("global_load_dwordx4 %0, %3, %4 offset:%c5 sc1\n\tglobal_load_dwordx4 %1, %3, %4 offset:%c6 sc1\n\tglobal_load_dwordx4 %2, %7, %4 offset:%c8 sc1\n\ts_waitcnt vmcnt(0)" \
      : "=&v"(ma), "=&v"(mb), "=&v"(sv) : "v"(lane * 16u), "s"(inb), "i"(((c) - 1) * 2048 - 3072), "i"(((c) - 1) * 2048 - 2048), "v"(c16 * 16u), "i"(3072 + ((c) - 1) * 256) : "memory")
#define RESEND(c, xa, xb, dp) asm volatile("global_store_dwordx4 %0, %1, %3 offset:%c4 sc1\n\tglobal_store_dwordx4 %0, %2, %3 offset:%c5 sc1\n\tglobal_store_dwordx4 %6, %7, %3 offset:%c8 sc1\n\ts_nop 1" \
      :: "v"(lane * 16u), "v"(xa), "v"(xb), "s"(dp), "i"(((c) - 1) * 2048 - 3072), "i"(((c) - 1) * 2048 - 2048), "v"(c16 * 16u), "v"(ml), "i"(3072 + ((c) - 1) * 256) : "memory")
#define COLLECT(c, ma, mb, sv, xa, xb, dp) do { \
    _Pragma("unroll 1") for (unsigned spin = 0; spin < SPIN_MAX && EMPTY(max(max(MAX4(ma), MAX4(mb)), MAX4(sv))); ++spin) LDMSG(c, ma, mb, sv); \
    if (!((rdy >> (c)) & 1u)) RESEND(c, xa, xb, dp); } while (0)
  {
    const unsigned e = max(max(MAX4(m3a), MAX4(m3b)), max(max(MAX4(s1), MAX4(s2)), MAX4(s3)));
    if (__builtin_expect(EMPTY(e) || (rdy & 14u) != 14u, 0)) {
      u32x4 ta, tb;
      COLLECT(3, m3a, m3b, s3, x3a, x3b, dp3);
      if (EMPTY(MAX4(s2)) || !(rdy & 4u)) { LDMSG(2, ta, tb, s2); COLLECT(2, ta, tb, s2, x2a, x2b, dp2); }
      if (EMPTY(MAX4(s1)) || !(rdy & 2u)) { LDMSG(1, ta, tb, s1); COLLECT(1, ta, tb, s1, x1a, x1b, dp1); }
    }
  }
  float w0[2], w1[2], w2[2], w3[2];
#pragma unroll
  for (int q = 0; q < 2; ++q) {
    const float a0 = __uint_as_float(ml[2 * q]), a1 = __uint_as_float(s1[2 * q]), a2 = __uint_as_float(s2[2 * q]), a3 = __uint_as_float(s3[2 * q]);
    const float M = fmaxf(fmaxf(a0, a1), fmaxf(a2, a3));
    const float e0 = __builtin_amdgcn_exp2f(a0 - M), e1 = __builtin_amdgcn_exp2f(a1 - M), e2 = __builtin_amdgcn_exp2f(a2 - M), e3 = __builtin_amdgcn_exp2f(a3 - M);
    const float inv = __builtin_amdgcn_rcpf(e0 * __uint_as_float(ml[2 * q + 1]) + e1 * __uint_as_float(s1[2 * q + 1]) + e2 * __uint_as_float(s2[2 * q + 1]) + e3 * __uint_as_float(s3[2 * q + 1]));
    w0[q] = e0 * inv; w1[q] = e1 * inv; w2[q] = e2 * inv; w3[q] = e3 * inv;
  }
  f32x16 acc;
#pragma unroll
  for (int i = 0; i < 8; ++i) {
    const unsigned d = i < 4 ? m3a[i & 3] : m3b[i & 3];
    acc[2 * i] = o0[2 * i] * w0[i >> 2] + __uint_as_float(d << 16) * w3[i >> 2];
    acc[2 * i + 1] = o0[2 * i + 1] * w0[i >> 2] + __uint_as_float(d & 0xffff0000u) * w3[i >> 2];
  }
  unsigned op = (qh * 32u + c16) * 256u + sp * 64u + (g4 & 1u) * 32u + h * 16u;
  asm volatile(
      ""
      "global_load_dwordx4 v[16:19], v233, %[in] offset:-3072 sc1\n\tglobal_load_dwordx4 v[20:23], v233, %[in] offset:-2048 sc1\n\t"
      "s_mov_b32 s40, %[lim]\n\t"
      "s_waitcnt vmcnt(2)\n\t"
      ".Lm2_%=:\n\t"
      "v_max3_u32 v96, v72, v73, v74\n\tv_max3_u32 v97, v75, v76, v77\n\tv_max3_u32 v96, v96, v78, v79\n\tv_max_u32 v96, v96, v97\n\tv_cmp_eq_u32 vcc, -1, v96\n\t"
      "s_cbranch_vccz .Lma_%=\n\t"
      "global_load_dwordx4 v[72:75], v233, %[in] offset:-1024 sc1\n\tglobal_load_dwordx4 v[76:79], v233, %[in] offset:0 sc1\n\t"
      "s_waitcnt vmcnt(0)\n\t"
      "s_sub_u32 s40, s40, 1\n\ts_cbranch_scc0 .Lm2_%=\n\t"
      ".Lma_%=:"
      : "+{v[72:75]}"(m2a), "+{v[76:79]}"(m2b), "={v[16:19]}"(m1a), "={v[20:23]}"(m1b), "+v"(acc), "+v"(op), "+v"(w1[0]), "+v"(w1[1]), "+v"(w2[0]), "+v"(w2[1])
      : "{v233}"(lane * 16u), [in] "s"(inb), [lim] "i"(SPIN_MAX) : "v96", "v97", "s40", "vcc", "scc", "memory");
#pragma unroll
  for (int i = 0; i < 8; ++i) {
    const unsigned b = i < 4 ? m2a[i & 3] : m2b[i & 3];
    acc[2 * i] += __uint_as_float(b << 16) * w2[i >> 2];
    acc[2 * i + 1] += __uint_as_float(b & 0xffff0000u) * w2[i >> 2];
  }
  asm volatile(
      "s_mov_b32 s40, %[lim]\n\t"
      ".Lp_%=:\n\t"
      "s_waitcnt vmcnt(0)\n\t"
      "v_max3_u32 v96, v16, v17, v18\n\tv_max3_u32 v97, v19, v20, v21\n\tv_max3_u32 v96, v96, v22, v23\n\tv_max_u32 v96, v96, v97\n\tv_cmp_eq_u32 vcc, -1, v96\n\t"
      "s_cbranch_vccz .Lpd_%=\n\t"
      "global_load_dwordx4 v[16:19], v233, %[in] offset:-3072 sc1\n\tglobal_load_dwordx4 v[20:23], v233, %[in] offset:-2048 sc1\n\t"
      "s_sub_u32 s40, s40, 1\n\ts_cbranch_scc0 .Lp_%=\n\t"
      "s_waitcnt vmcnt(0)\n\t"
      ".Lpd_%=:"
      : "+{v[16:19]}"(m1a), "+{v[20:23]}"(m1b), "+v"(acc)
      : "{v233}"(lane * 16u), [in] "s"(inb), [lim] "i"(SPIN_MAX) : "v96", "v97", "s40", "vcc", "scc", "memory");
  unsigned ob[8];
#pragma unroll
  for (int i = 0; i < 8; ++i) {
    const unsigned a = i < 4 ? m1a[i & 3] : m1b[i & 3];
    ob[i] = cvtpk(acc[2 * i] + __uint_as_float(a << 16) * w1[i >> 2], acc[2 * i + 1] + __uint_as_float(a & 0xffff0000u) * w1[i >> 2]);
  }
#pragma unroll
  for (int k = 0; k < 2; ++k) {
    auto x = __builtin_amdgcn_permlane16_swap(ob[4 * k], ob[4 * k + 2], false, false);
    auto y = __builtin_amdgcn_permlane16_swap(ob[4 * k + 1], ob[4 * k + 3], false, false);
    u32x4 u = {x[0], y[0], x[1], y[1]};
    *(u32x4*)(O + 2048 + op + (4096 * k - 2048)) = u;
  }
}

extern "C" void kernel_launch(void* const* d_in, const int* in_sizes, int n_in, void* d_out, int out_size, void* d_ws, size_t ws_size, hipStream_t stream) {
  (void)hipFuncSetAttribute((const void*)gqa_kernel, hipFuncAttributeMaxDynamicSharedMemorySize, LDS_BYTES);
  hipLaunchKernelGGL(gqa_kernel, dim3(256), dim3(256), LDS_BYTES, stream, (const char*)d_in[0], (const char*)d_in[1], (const char*)d_in[2], (char*)d_out, (char*)d_ws);
}
```
